```python
import torch

def _softmax(x, axis=-1):
    return torch.softmax(x, dim=axis)

def _gelu(x):
    return torch.nn.functional.gelu(x, approximate='tanh')

def _silu(x):
    return torch.nn.functional.silu(x)

def _sigmoid(x):
    return torch.sigmoid(x)

def _layernorm(x, axis=-1, eps=1e-05):
    m = x.mean(dim=axis, keepdim=True)
    v = x.var(dim=axis, keepdim=True, unbiased=False)
    return (x - m) / torch.sqrt(v + eps)

def _scatter_add(tgt, idx, src, axis=0):
    return tgt.clone().index_add_(axis, idx, src)

def _segment_sum(src, seg, n):
    out = torch.zeros((n,) + src.shape[1:], dtype=src.dtype)
    return out.index_add_(0, seg, src)

def _pad(x, pw):
    flat = tuple((v for pair in reversed(pw) for v in pair))
    return torch.nn.functional.pad(x, flat)
kernel_name = 'adaLN_cond__memory_aug_recurrent_mem_tr__dense_activation_gelu__prompt'

def setup_inputs():

    _w = lambda a: (lambda t: t.to(torch.bfloat16) if t.is_floating_point() else t)(torch.as_tensor(a))

    def _A_setup():
        B, S, Dm = (1, 2048, 512)
        H, Dh = (32, 128)
        M, seg = (8, 32)
        seg = min(seg, S)
        S = S // seg * seg
        return {'x': _w((lambda *s: torch.randn(*s, dtype=torch.bfloat16) * 0.05)(B, S, Dm)), 'mem0': _w((lambda *s: torch.randn(*s, dtype=torch.bfloat16) * 0.8)(B, M, Dm)), 'Wq': _w((lambda *s: torch.randn(*s, dtype=torch.bfloat16) * 0.8)(Dm, H, Dh) * Dm ** (-0.5)), 'Wk': _w((lambda *s: torch.randn(*s, dtype=torch.bfloat16) * 0.8)(Dm, H, Dh) * Dm ** (-0.5)), 'Wv': _w((lambda *s: torch.randn(*s, dtype=torch.bfloat16) * 0.8)(Dm, H, Dh) * Dm ** (-0.5)), 'Wo': _w((lambda *s: torch.randn(*s, dtype=torch.bfloat16) * 0.8)(H, Dh, Dm) * (H * Dh) ** (-0.5)), 'seg_len': seg}
    _A_in = _A_setup()

    def _B_setup():
        B, S, D, F = (1, 2048, 512, 1792)
        return {'x': _w((lambda *s: torch.randn(*s, dtype=torch.bfloat16) * 0.05)(B, S, D)), 'w1': _w((lambda *s: torch.randn(*s, dtype=torch.bfloat16) * 0.8)(D, F) * D ** (-0.5)), 'b1': _w((lambda *s: torch.randn(*s, dtype=torch.bfloat16) * 12.8)(F) * 0.01), 'w2': _w((lambda *s: torch.randn(*s, dtype=torch.bfloat16) * 0.4)(F, D) * F ** (-0.5)), 'b2': _w((lambda *s: torch.randn(*s, dtype=torch.bfloat16) * 6.4)(D) * 0.01)}
    _B_in = _B_setup()
    return {'A_x': _A_in['x'], 'A_mem0': _A_in['mem0'], 'A_Wq': _A_in['Wq'], 'A_Wk': _A_in['Wk'], 'A_Wv': _A_in['Wv'], 'A_Wo': _A_in['Wo'], 'A_seg_len': _A_in['seg_len'], 'B_x': _B_in['x'], 'B_w1': _B_in['w1'], 'B_b1': _B_in['b1'], 'B_w2': _B_in['w2'], 'B_b2': _B_in['b2'], 'c': _w((lambda *s: torch.randn(*s, dtype=torch.bfloat16) * 0.1)(1, 512) * 8.0), 'w_mod': _w((lambda *s: torch.randn(*s, dtype=torch.bfloat16) * 0.1)(512, 6 * 512) * 512 ** (-0.5) * 8.0)}

def reference(A_x, A_mem0, A_Wq, A_Wk, A_Wv, A_Wo, A_seg_len, B_x, B_w1, B_b1, B_w2, B_b2, c, w_mod):

    def _A_ref(x, mem0, Wq, Wk, Wv, Wo, seg_len):
        B, S, Dm = x.shape
        H, Dh = (Wq.shape[1], Wq.shape[2])
        M = mem0.shape[1]
        scale = Dh ** (-0.5)
        n_seg = S // seg_len
        L = M + seg_len
        idx = torch.arange(L)
        mask = (idx[:, None] < M) | (idx[:, None] >= idx[None, :])
        mem = mem0
        outs = []
        for s in range(n_seg):
            xs = x[:, s * seg_len:(s + 1) * seg_len, :].clone()
            h = torch.concatenate([mem, xs], axis=1)
            q = torch.einsum('btd,dhe->bhte', h, Wq)
            k = torch.einsum('btd,dhe->bhte', h, Wk)
            v = torch.einsum('btd,dhe->bhte', h, Wv)
            scores = torch.einsum('bhqe,bhke->bhqk', q, k) * scale
            scores = torch.where(mask[None, None], scores, -1000000000.0)
            p = _softmax(scores, axis=-1)
            o = torch.einsum('bhqk,bhke->bhqe', p, v)
            o = torch.einsum('bhte,hed->btd', o, Wo)
            mem = o[:, :M, :].clone()
            outs.append(o[:, M:, :])
        return torch.concatenate(outs, axis=1)

    def _B_ref(x, w1, b1, w2, b2):
        h = torch.einsum('bsd,df->bsf', x, w1) + b1
        h = _gelu(h)
        return torch.einsum('bsf,fd->bsd', h, w2) + b2
    x = A_x
    mod = (_silu(c) @ w_mod).reshape(c.shape[0], 6, 1, -1)
    sa, ca, ga, sb, cb, gb = (mod[:, 0].clone(), mod[:, 1].clone(), mod[:, 2].clone(), mod[:, 3].clone(), mod[:, 4].clone(), mod[:, 5].clone())
    h = _layernorm(x, axis=-1) * (1 + ca) + sa
    x = x + ga * _A_ref(h, A_mem0, A_Wq, A_Wk, A_Wv, A_Wo, A_seg_len)
    h = _layernorm(x, axis=-1) * (1 + cb) + sb
    x = x + gb * _B_ref(h, B_w1, B_b1, B_w2, B_b2)
    return x
```

```cpp
#include <hip/hip_runtime.h>
#include <hip/hip_bf16.h>
#include <stdint.h>

typedef __bf16 bf16;
typedef __bf16 bf16x2 __attribute__((ext_vector_type(2)));
typedef __bf16 bf16x8 __attribute__((ext_vector_type(8)));
typedef __bf16 bf16x16 __attribute__((ext_vector_type(16)));
typedef short s16x4 __attribute__((ext_vector_type(4)));
typedef float f32x2 __attribute__((ext_vector_type(2)));
typedef float f32x4 __attribute__((ext_vector_type(4)));
typedef unsigned u32x2 __attribute__((ext_vector_type(2)));
typedef unsigned u32x4 __attribute__((ext_vector_type(4)));
typedef unsigned u32x8 __attribute__((ext_vector_type(8)));
#define GAS __attribute__((address_space(1)))
#define LAS __attribute__((address_space(3)))

constexpr int S = 2048, DM = 512, NH = 32, DH = 128, NM = 8, SEG = 32, NSEG = 64, FF = 1792;

__device__ __forceinline__ float bf2f(unsigned short u) { return __uint_as_float((unsigned)u << 16); }
__device__ __forceinline__ float bflo(unsigned u) { return __uint_as_float(u << 16); }
__device__ __forceinline__ float bfhi(unsigned u) { return __uint_as_float(u & 0xffff0000u); }
__device__ __forceinline__ unsigned pk2(float a, float b) {
  f32x2 p = {a, b};
  bf16x2 r = __builtin_convertvector(p, bf16x2);
  return __builtin_bit_cast(unsigned, r);
}
__device__ __forceinline__ float rbf(float a) { return bflo(pk2(a, a)); }
__device__ __forceinline__ f32x4 mfma32(u32x4 a, u32x4 b, f32x4 c) {
  return __builtin_amdgcn_mfma_f32_16x16x32_bf16(__builtin_bit_cast(bf16x8, a), __builtin_bit_cast(bf16x8, b), c, 0, 0, 0);
}
__device__ __forceinline__ f32x4 mfma16(u32x2 a, u32x2 b, f32x4 c) {
  return mfma32((u32x4){a[0], a[1], 0u, 0u}, (u32x4){b[0], b[1], 0u, 0u}, c);
}
__device__ __forceinline__ f32x4 smf(u32x4 a, u32x8 b, f32x4 c, int idx) {
  return __builtin_amdgcn_smfmac_f32_16x16x64_bf16(__builtin_bit_cast(bf16x8, a), __builtin_bit_cast(bf16x16, b), c, idx, 0, 0);
}
__device__ __forceinline__ u32x8 cat8(u32x4 a, u32x4 b) {
  return __builtin_shufflevector(a, b, 0, 1, 2, 3, 4, 5, 6, 7);
}
__device__ __forceinline__ float xmax16(float v) {
  auto r = __builtin_amdgcn_permlane16_swap(__float_as_uint(v), __float_as_uint(v), false, false);
  return fmaxf(__uint_as_float(r[0]), __uint_as_float(r[1]));
}
__device__ __forceinline__ float xmax32(float v) {
  auto r = __builtin_amdgcn_permlane32_swap(__float_as_uint(v), __float_as_uint(v), false, false);
  return fmaxf(__uint_as_float(r[0]), __uint_as_float(r[1]));
}
__device__ __forceinline__ float xadd16(float v) {
  auto r = __builtin_amdgcn_permlane16_swap(__float_as_uint(v), __float_as_uint(v), false, false);
  return __uint_as_float(r[0]) + __uint_as_float(r[1]);
}
__device__ __forceinline__ float xadd32(float v) {
  auto r = __builtin_amdgcn_permlane32_swap(__float_as_uint(v), __float_as_uint(v), false, false);
  return __uint_as_float(r[0]) + __uint_as_float(r[1]);
}

constexpr size_t MB = 1u << 20;
constexpr size_t WS_CTL = 0;
constexpr size_t WS_FLAGS = 0;
constexpr size_t WS_ERR = 256 * 1024;
constexpr size_t CTL_ZERO_BYTES = 256 * 1024 + 256;
constexpr size_t WS_PART = 1 * MB;
constexpr size_t WS_MEMX = 2 * MB;
constexpr size_t RING_BYTES = 1 * MB + 256 * 1024;
constexpr size_t WS_MOD = 3 * MB;
constexpr size_t WS_H = 4 * MB;
constexpr size_t WS_X1 = 6 * MB;
constexpr size_t WS_H2 = 8 * MB;
constexpr size_t WS_G = 10 * MB;
constexpr size_t WS_WQF = 18 * MB, WS_WKF = 22 * MB, WS_WVF = 26 * MB, WS_WOF = 30 * MB, WS_WOC = 34 * MB;
constexpr size_t WS_W1F = 38 * MB, WS_W2F = 40 * MB;
constexpr size_t WS_QF = 48 * MB, WS_KF = 64 * MB, WS_VF = 80 * MB, WS_O = 96 * MB, WS_KMF = 112 * MB, WS_VMF = 120 * MB;

__global__ void k_prep_frag(const unsigned short* W, int K, int N, u32x4* out) {
  int id = blockIdx.x * 256 + threadIdx.x;
  int nks = K / 32;
  if (id >= (N / 16) * nks * 64) return;
  int lane = id & 63, ks = (id >> 6) % nks, nt = (id >> 6) / nks;
  int c = lane & 15, g = lane >> 4;
  unsigned v[4];
#pragma unroll
  for (int j = 0; j < 4; ++j) {
    unsigned lo = W[(size_t)(32 * ks + 8 * g + 2 * j) * N + 16 * nt + c];
    unsigned hi = W[(size_t)(32 * ks + 8 * g + 2 * j + 1) * N + 16 * nt + c];
    v[j] = lo | (hi << 16);
  }
  out[id] = (u32x4){v[0], v[1], v[2], v[3]};
}
__global__ void k_prep_woc(const unsigned short* Wo, u32x4* out) {
  int id = blockIdx.x * 256 + threadIdx.x;
  int lane = id & 63, hh = (id >> 6) & 1, ks = (id >> 7) & 1, T = (id >> 8) & 7, w = (id >> 11) & 3, h = id >> 13;
  int c = lane & 15, gb = lane >> 4;
  int p = 128 * w + 64 * (T >> 2) + 4 * c + (T & 3);
  int pks = p >> 6, grp = (p >> 3) & 7, ja = p & 7;
  int d = 64 * pks + 16 * (grp >> 1) + 4 * (ja >> 1) + 2 * (grp & 1) + (ja & 1);
  unsigned v[4];
#pragma unroll
  for (int jj = 0; jj < 4; ++jj) {
    unsigned x[2];
#pragma unroll
    for (int u = 0; u < 2; ++u) {
      int k = 32 * hh + 8 * gb + 2 * jj + u;
      int e = 64 * ks + 16 * (k >> 4) + 8 * ((k >> 1) & 1) + 2 * ((k >> 2) & 3) + (k & 1);
      x[u] = Wo[(size_t)(h * DH + e) * DM + d];
    }
    v[jj] = x[0] | (x[1] << 16);
  }
  out[id] = (u32x4){v[0], v[1], v[2], v[3]};
}

__global__ void k_mod(const unsigned short* c, const unsigned short* wmod, float* mod) {
  __shared__ float sc[512];
  __shared__ float red[4][64];
  for (int i = threadIdx.x; i < 512; i += 256) {
    float x = bf2f(c[i]);
    sc[i] = rbf(x / (1.0f + __expf(-x)));
  }
  __syncthreads();
  int col = blockIdx.x * 64 + (threadIdx.x & 63), w = threadIdx.x >> 6;
  float acc = 0.f;
  for (int k = w * 128; k < w * 128 + 128; ++k) acc += sc[k] * bf2f(wmod[(size_t)k * 3072 + col]);
  red[w][threadIdx.x & 63] = acc;
  __syncthreads();
  if (w == 0) mod[col] = rbf(red[0][threadIdx.x] + red[1][threadIdx.x] + red[2][threadIdx.x] + red[3][threadIdx.x]);
}

__global__ void k_ln(const unsigned short* x, const float* ss, const float* cc, unsigned short* out) {
  int row = blockIdx.x * 4 + (threadIdx.x >> 6), lane = threadIdx.x & 63;
  u32x4 v = *(const u32x4*)(x + (size_t)row * DM + lane * 8);
  float f[8] = {bflo(v[0]), bfhi(v[0]), bflo(v[1]), bfhi(v[1]), bflo(v[2]), bfhi(v[2]), bflo(v[3]), bfhi(v[3])};
  float s = 0.f;
#pragma unroll
  for (int j = 0; j < 8; ++j) s += f[j];
  for (int o = 1; o < 64; o <<= 1) s += __shfl_xor(s, o);
  float m = s * (1.0f / DM), q = 0.f;
#pragma unroll
  for (int j = 0; j < 8; ++j) q += (f[j] - m) * (f[j] - m);
  for (int o = 1; o < 64; o <<= 1) q += __shfl_xor(q, o);
  float mb = rbf(m), sd = rbf(sqrtf(rbf(rbf(q * (1.0f / DM)) + 1e-5f)));
  float r[8];
#pragma unroll
  for (int j = 0; j < 8; ++j) {
    float n = rbf(rbf(f[j] - mb) / sd);
    r[j] = rbf(n * rbf(1.0f + cc[lane * 8 + j])) + ss[lane * 8 + j];
  }
  *(u32x4*)(out + (size_t)row * DM + lane * 8) = (u32x4){pk2(r[0], r[1]), pk2(r[2], r[3]), pk2(r[4], r[5]), pk2(r[6], r[7])};
}

__global__ void __launch_bounds__(256) k_qkv(const unsigned short* h, const u32x4* wq, const u32x4* wk, const u32x4* wv,
                                             u32x4* Qf, u32x4* Kf, u32x4* Vf) {
  int s = blockIdx.x >> 5, hd = blockIdx.x & 31, lane = threadIdx.x & 63, w = threadIdx.x >> 6;
  int c = lane & 15, g = lane >> 4;
  f32x4 aq[2][2] = {}, ak[2][2] = {}, av[2][2] = {};
  for (int ks = 0; ks < 16; ++ks) {
    u32x4 a[2];
#pragma unroll
    for (int mt = 0; mt < 2; ++mt) a[mt] = *(const u32x4*)(h + (size_t)(s * 32 + mt * 16 + c) * DM + ks * 32 + g * 8);
#pragma unroll
    for (int T = 0; T < 2; ++T) {
      size_t o = ((size_t)(8 * hd + 2 * w + T) * 16 + ks) * 64 + lane;
      u32x4 bq = wq[o], bk = wk[o], bv = wv[o];
#pragma unroll
      for (int mt = 0; mt < 2; ++mt) {
        aq[T][mt] = mfma32(bq, a[mt], aq[T][mt]);
        ak[T][mt] = mfma32(bk, a[mt], ak[T][mt]);
        av[T][mt] = mfma32(a[mt], bv, av[T][mt]);
      }
    }
  }
  size_t base = (size_t)(s * 32 + hd) * 512;
#pragma unroll
  for (int mt = 0; mt < 2; ++mt) {
    Qf[base + (mt * 4 + w) * 64 + lane] = (u32x4){pk2(aq[0][mt][0], aq[0][mt][1]), pk2(aq[0][mt][2], aq[0][mt][3]), pk2(aq[1][mt][0], aq[1][mt][1]), pk2(aq[1][mt][2], aq[1][mt][3])};
    Kf[base + (mt * 4 + w) * 64 + lane] = (u32x4){pk2(ak[0][mt][0], ak[0][mt][1]), pk2(ak[0][mt][2], ak[0][mt][3]), pk2(ak[1][mt][0], ak[1][mt][1]), pk2(ak[1][mt][2], ak[1][mt][3])};
  }
#pragma unroll
  for (int T = 0; T < 2; ++T)
    Vf[base + (2 * w + T) * 64 + lane] = (u32x4){pk2(av[T][0][0], av[T][0][1]), pk2(av[T][0][2], av[T][0][3]), pk2(av[T][1][0], av[T][1][1]), pk2(av[T][1][2], av[T][1][3])};
}

struct ChainArgs {
  const u32x4 *wq, *wk, *wv, *woc;
  const unsigned short* mem0;
  const u32x4 *Kf, *Vf;
  u32x4* KMf;
  u32x2* VMf;
  char* ws;
  int V1, V2, V3, pad;
};
#define RT() ((unsigned)__builtin_amdgcn_s_memrealtime())
#define BAR() asm volatile("s_waitcnt lgkmcnt(0)\n\ts_barrier" ::: "memory")
constexpr unsigned SENT = 0xffffffffu;
constexpr unsigned SPIN = 20000;
constexpr int L_MEM = 0, L_KX = L_MEM + 8192, L_Q = L_KX + 8192, L_KM = L_Q + 2048, L_O = L_KM + 4096, L_WO = L_O + 2048, L_END = L_WO + 131072;
constexpr float SCL = 0.08838834764831845f * 1.4426950408889634f;

__device__ __forceinline__ unsigned max3u(unsigned a, unsigned b, unsigned c) { return max(max(a, b), c); }
__device__ __forceinline__ unsigned max4v(u32x4 v, unsigned m) { return max3u(max3u(m, v[0], v[1]), v[2], v[3]); }
__device__ __forceinline__ unsigned flag_max(unsigned v, unsigned ref) {
  int d = (int)(v - ref);
  d = max(d, __builtin_amdgcn_update_dpp(d, d, 0x121, 0xf, 0xf, false));
  d = max(d, __builtin_amdgcn_update_dpp(d, d, 0x122, 0xf, 0xf, false));
  d = max(d, __builtin_amdgcn_update_dpp(d, d, 0x124, 0xf, 0xf, false));
  d = max(d, __builtin_amdgcn_update_dpp(d, d, 0x128, 0xf, 0xf, false));
  int a = __builtin_amdgcn_readlane(d, 0), b = __builtin_amdgcn_readlane(d, 16);
  return ref + (unsigned)max(a, b);
}

__global__ void __launch_bounds__(256, 1) k_chain(ChainArgs a) {
  extern __shared__ __attribute__((aligned(16))) char lds[];
  const int lane = threadIdx.x & 63, w = __builtin_amdgcn_readfirstlane(threadIdx.x >> 6);
  const int c = lane & 15, g = lane >> 4;
  auto rs = __builtin_amdgcn_make_buffer_rsrc(a.ws, 0, 4 * MB, 0x00020000);
  GAS unsigned* err = (GAS unsigned*)(a.ws + WS_ERR);
  if (blockIdx.x >= 32) {
    if (w >= 2) return;
    const int r = (blockIdx.x - 32) * 2 + w;
    u32x4 sel;
    {
      int sg = 2 * (c & 3) + ((c >> 2) & 1);
#pragma unroll
      for (int j = 0; j < 4; ++j) sel[j] = (sg == 2 * j) ? 0x3f80u : ((sg == 2 * j + 1) ? 0x3f800000u : 0u);
    }
    const u32x4 sent = {SENT, SENT, SENT, SENT};
    const unsigned tq = lane & 7;
    const unsigned ooff = r * 128 + (2 * (tq & 3) + (g & 1)) * 16 + (tq >> 2) * 8 + (((lane >> 3) & 1) + 2 * (g >> 1)) * 8192;
    unsigned tf = RT();
    for (int s = 0; s < NSEG - 1; ++s) {
      const unsigned tag = s + 1, slot = s & 3, nslot = (s + 1) & 3;
      while ((int)(RT() - tf) < 0) {}
      u32x2 f;
      for (unsigned spins = 0;; ++spins) {
        asm volatile("" ::: "memory");
        f = __builtin_amdgcn_raw_buffer_load_b64(rs, WS_FLAGS + s * 4096 + (lane & 31) * 128, 0, 16);
        if (__all(f[1] == tag)) break;
        if (spins > SPIN * 16) { *err = 1; break; }
        __builtin_amdgcn_s_sleep(1);
      }
      const unsigned tau = flag_max(f[0], RT()) + a.V1;
      tf = tau + a.V2 + a.V3;
      while ((int)(RT() - tau) < 0) {}
      f32x4 acc;
      for (unsigned spins = 0;; ++spins) {
        asm volatile("" ::: "memory");
        u32x4 v[4];
#pragma unroll
        for (int k = 0; k < 4; ++k) v[k] = __builtin_amdgcn_raw_buffer_load_b128(rs, WS_PART + slot * 262144 + r * 4096 + k * 1024 + lane * 16, 0, 16);
        acc = (f32x4){0.f, 0.f, 0.f, 0.f};
        unsigned m = 0;
#pragma unroll
        for (int k = 0; k < 4; ++k) { acc = mfma32(sel, v[k], acc); m = max4v(v[k], m); }
        if (__all(m != SENT)) break;
        if (spins > SPIN) { *err = 2; break; }
      }
      float o[4];
#pragma unroll
      for (int j = 0; j < 4; ++j) o[j] = acc[j] + __uint_as_float(__builtin_amdgcn_update_dpp(0, __float_as_uint(acc[j]), 0x128, 0xf, 0xf, false));
      u32x2 ov = {min(pk2(o[0], o[1]), SENT - 1), min(pk2(o[2], o[3]), SENT - 1)};
      __builtin_amdgcn_raw_buffer_store_b64(ov, rs, WS_MEMX + nslot * 65536 + ooff, 0, 16);
      __builtin_amdgcn_raw_buffer_store_b64(ov, rs, WS_MEMX + nslot * 65536 + 32768 + ooff, 0, 16);
      __builtin_amdgcn_s_sleep(8);
#pragma unroll
      for (int k = 0; k < 4; ++k) __builtin_amdgcn_raw_buffer_store_b128(sent, rs, WS_PART + slot * 262144 + r * 4096 + k * 1024 + lane * 16, 0, 16);
      __builtin_amdgcn_raw_buffer_store_b128(sent, rs, WS_MEMX + ((s + 3) & 3) * 65536 + (lane >> 3) * 8192 + r * 128 + (lane & 7) * 16, 0, 16);
    }
    return;
  }
  const int h = blockIdx.x;
  u32x8 WQ[2][8], WK[2][8], WV[2][8];
#pragma unroll
  for (int T = 0; T < 2; ++T)
#pragma unroll
    for (int ks = 0; ks < 8; ++ks) {
      size_t o = ((size_t)(8 * h + 2 * w + T) * 16 + 2 * ks) * 64 + lane;
      WQ[T][ks] = cat8(a.wq[o], a.wq[o + 64]);
      WK[T][ks] = cat8(a.wk[o], a.wk[o + 64]);
      WV[T][ks] = cat8(a.wv[o], a.wv[o + 64]);
    }
  for (int i = threadIdx.x; i < 8192; i += 256) *(LAS u32x4*)(uintptr_t)(L_WO + i * 16 + (unsigned)(uintptr_t)(LAS char*)lds) = a.woc[(size_t)h * 8192 + i];
  LAS char* L = (LAS char*)lds;
  *(LAS u32x4*)(L + L_KM + threadIdx.x * 16) = (u32x4){0, 0, 0, 0};
#pragma unroll
  for (int u = 0; u < 2; ++u) {
    int ks = 2 * w + u, t = c >> 1, b = c & 1;
    u32x4 v;
#pragma unroll
    for (int j = 0; j < 4; ++j) v[j] = min(*(const unsigned*)(a.mem0 + t * DM + 64 * ks + 16 * g + 4 * j + 2 * b), SENT - 1);
    *(LAS u32x4*)(L + L_MEM + ks * 1024 + lane * 16) = v;
  }
  const int idx = (lane & 1) ? 0xEEEE : 0x4444;
  const unsigned msrc = (2 * g + (c & 1)) * 128 + (c >> 1) * 16 + (h & 7) * 8192;
  const unsigned poff = (16 * w + (c >> 1)) * 4096 + h * 128 + ((c & 1) * 4 + g) * 16;
  const unsigned trq = L_Q + (4 * g + (c >> 2)) * 16 + (c & 1) * 8;
  const unsigned trk = L_KM + (4 * g + (c >> 2)) * 32 + (c & 3) * 8;
  unsigned owr;
  {
    int t = lane & 7, T = (lane >> 3) & 1, grp = 4 * (w & 1) + 2 * T + (g >> 1);
    owr = L_O + (w >> 1) * 1024 + (16 * (grp >> 1) + 2 * t + (grp & 1)) * 16 + (g & 1) * 8;
  }
  u32x4 VX[2];
  {
    const u32x4* kf = a.Kf + (size_t)h * 512;
#pragma unroll
    for (int u = 0; u < 2; ++u)
      __builtin_amdgcn_global_load_lds((const GAS void*)(kf + (2 * w + u) * 64 + lane), (LAS void*)(L + L_KX + (2 * w + u) * 1024), 16, 0, 0);
#pragma unroll
    for (int T = 0; T < 2; ++T) VX[T] = a.Vf[(size_t)h * 512 + (2 * w + T) * 64 + lane];
  }
  unsigned Cprev = 90;
  for (int s = 0;; ++s) {
    const unsigned tag = s + 1;
    u32x4 A[8];
    f32x4 q0, q1;
    unsigned ta;
    for (unsigned spins = 0;; ++spins) {
      asm volatile("s_waitcnt vmcnt(0)" ::: "memory");
      BAR();
      ta = RT();
#pragma unroll
      for (int ks = 0; ks < 8; ++ks) A[ks] = *(LAS u32x4*)(L + L_MEM + ks * 1024 + lane * 16);
      q0 = q1 = (f32x4){0.f, 0.f, 0.f, 0.f};
      unsigned m = 0;
#pragma unroll
      for (int ks = 0; ks < 8; ++ks) {
        q0 = smf(A[ks], WQ[0][ks], q0, idx);
        q1 = smf(A[ks], WQ[1][ks], q1, idx);
        m = max4v(A[ks], m);
      }
      if (__all(m != SENT)) break;
      if (spins > SPIN) { *err = 3; break; }
      BAR();
#pragma unroll
      for (int u = 0; u < 2; ++u)
        __builtin_amdgcn_global_load_lds((const GAS void*)(a.ws + WS_MEMX + (s & 3) * 65536 + msrc + (2 * w + u) * 1024), (LAS void*)(L + L_MEM + (2 * w + u) * 1024), 16, 0, 16);
    }
    if (threadIdx.x == 0) {
      u32x2 f = {ta + Cprev, tag};
      __builtin_amdgcn_raw_buffer_store_b64(f, rs, WS_FLAGS + s * 4096 + h * 128, 0, 16);
    }
    f32x4 k0 = {0.f, 0.f, 0.f, 0.f}, k1 = k0;
#pragma unroll
    for (int ks = 0; ks < 8; ++ks) {
      k0 = smf(A[ks], WK[0][ks], k0, idx);
      k1 = smf(A[ks], WK[1][ks], k1, idx);
      if (ks == 1) {
        *(LAS unsigned*)(L + L_Q + (32 * w + c) * 16 + g * 4) = pk2(q0[0] + q0[1], q0[2] + q0[3]);
        *(LAS unsigned*)(L + L_Q + (32 * w + 16 + c) * 16 + g * 4) = pk2(q1[0] + q1[1], q1[2] + q1[3]);
      }
      if (ks == 3) BAR();
    }
    u32x4 QF[4];
#pragma unroll
    for (int kk = 0; kk < 4; ++kk) {
      u32x2 x0 = __builtin_bit_cast(u32x2, __builtin_amdgcn_ds_read_tr16_b64_v4i16((LAS s16x4*)(L + trq + (32 * kk) * 16)));
      u32x2 x1 = __builtin_bit_cast(u32x2, __builtin_amdgcn_ds_read_tr16_b64_v4i16((LAS s16x4*)(L + trq + (32 * kk + 16) * 16)));
      QF[kk] = (u32x4){x0[0], x0[1], x1[0], x1[1]};
    }
    f32x4 s1 = {0.f, 0.f, 0.f, 0.f}, s2 = s1, s0 = s1;
#pragma unroll
    for (int kk = 0; kk < 4; ++kk) {
      s1 = mfma32(*(LAS u32x4*)(L + L_KX + kk * 1024 + lane * 16), QF[kk], s1);
      s2 = mfma32(*(LAS u32x4*)(L + L_KX + (4 + kk) * 1024 + lane * 16), QF[kk], s2);
      if (kk == 1) {
        *(LAS unsigned*)(L + L_KM + (32 * w + c) * 32 + g * 8) = pk2(k0[0] + k0[1], k0[2] + k0[3]);
        *(LAS unsigned*)(L + L_KM + (32 * w + 16 + c) * 32 + g * 8) = pk2(k1[0] + k1[1], k1[2] + k1[3]);
      }
    }
    BAR();
    f32x4 v0 = {0.f, 0.f, 0.f, 0.f}, v1 = v0;
    u32x4 KM[4];
#pragma unroll
    for (int kk = 0; kk < 4; ++kk) {
      u32x2 x0 = __builtin_bit_cast(u32x2, __builtin_amdgcn_ds_read_tr16_b64_v4i16((LAS s16x4*)(L + trk + (32 * kk) * 32)));
      u32x2 x1 = __builtin_bit_cast(u32x2, __builtin_amdgcn_ds_read_tr16_b64_v4i16((LAS s16x4*)(L + trk + (32 * kk + 16) * 32)));
      KM[kk] = (u32x4){x0[0], x0[1], x1[0], x1[1]};
    }
#pragma unroll
    for (int ks = 0; ks < 3; ++ks) {
      v0 = smf(A[ks], WV[0][ks], v0, idx);
      v1 = smf(A[ks], WV[1][ks], v1, idx);
    }
#pragma unroll
    for (int kk = 0; kk < 4; ++kk) s0 = mfma32(KM[kk], QF[kk], s0);
#pragma unroll
    for (int ks = 3; ks < 8; ++ks) {
      v0 = smf(A[ks], WV[0][ks], v0, idx);
      v1 = smf(A[ks], WV[1][ks], v1, idx);
    }
    float mx = fmaxf(fmaxf(fmaxf(s1[0], s1[1]), fmaxf(s1[2], s1[3])), fmaxf(fmaxf(s2[0], s2[1]), fmaxf(s2[2], s2[3])));
    mx = fmaxf(mx, fmaxf(s0[0], s0[1]));
    mx = xmax32(xmax16(mx));
    const float nm = -mx * SCL;
    float p0[2], p1[4], p2[4];
#pragma unroll
    for (int j = 0; j < 2; ++j) p0[j] = __builtin_amdgcn_exp2f(fmaf(s0[j], SCL, nm));
#pragma unroll
    for (int j = 0; j < 4; ++j) { p1[j] = __builtin_amdgcn_exp2f(fmaf(s1[j], SCL, nm)); p2[j] = __builtin_amdgcn_exp2f(fmaf(s2[j], SCL, nm)); }
    const u32x4 PX = {pk2(p1[0], p1[1]), pk2(p1[2], p1[3]), pk2(p2[0], p2[1]), pk2(p2[2], p2[3])};
    const u32x2 PM = {pk2(p0[0], p0[1]), 0u};
    const u32x2 vm = {pk2(v0[0] + v0[1], v0[2] + v0[3]), pk2(v1[0] + v1[1], v1[2] + v1[3])};
    {
      u32x4 kmw = w == 0 ? KM[0] : (w == 1 ? KM[1] : (w == 2 ? KM[2] : KM[3]));
      a.KMf[((size_t)(s * 32 + h) * 4 + w) * 64 + lane] = kmw;
      a.VMf[((size_t)(s * 32 + h) * 4 + w) * 64 + lane] = vm;
    }
    if (s == NSEG - 1) break;
    f32x4 o0 = {0.f, 0.f, 0.f, 0.f}, o1 = o0;
    o0 = mfma32(VX[0], PX, o0);
    o1 = mfma32(VX[1], PX, o1);
    o0 = mfma16((u32x2){vm[0], 0u}, PM, o0);
    o1 = mfma16((u32x2){vm[1], 0u}, PM, o1);
    float sum = (bflo(PX[0]) + bfhi(PX[0])) + (bflo(PX[1]) + bfhi(PX[1])) + (bflo(PX[2]) + bfhi(PX[2])) + (bflo(PX[3]) + bfhi(PX[3])) + (bflo(PM[0]) + bfhi(PM[0]));
    sum = xadd32(xadd16(sum));
    const float rinv = __builtin_amdgcn_rcpf(sum);
    u32x2 fl = __builtin_amdgcn_raw_buffer_load_b64(rs, WS_FLAGS + s * 4096 + (lane & 31) * 128, 0, 16);
    {
      f32x4 oo = (lane & 8) ? o1 : o0;
      *(LAS u32x2*)(L + owr) = (u32x2){pk2(oo[0] * rinv, oo[1] * rinv), pk2(oo[2] * rinv, oo[3] * rinv)};
    }
    BAR();
    const u32x4 OA0 = *(LAS u32x4*)(L + L_O + lane * 16), OA1 = *(LAS u32x4*)(L + L_O + 1024 + lane * 16);
#pragma unroll
    for (int i = 0; i < 2; ++i) {
      u32x4 pv;
#pragma unroll
      for (int t = 0; t < 4; ++t) {
        const int T = 4 * i + t;
        LAS char* wp = L + L_WO + w * 32768 + T * 4096 + lane * 16;
        f32x4 acc = {0.f, 0.f, 0.f, 0.f};
        acc = smf(OA0, cat8(*(LAS u32x4*)(wp), *(LAS u32x4*)(wp + 1024)), acc, idx);
        acc = smf(OA1, cat8(*(LAS u32x4*)(wp + 2048), *(LAS u32x4*)(wp + 3072)), acc, idx);
        pv[t] = min(pk2(acc[0] + acc[1], acc[2] + acc[3]), SENT - 1);
      }
      __builtin_amdgcn_raw_buffer_store_b128(pv, rs, WS_PART + (s & 3) * 262144 + i * 32768 + poff, 0, 16);
    }
    const unsigned tp = RT();
    Cprev = tp - ta;
    {
      const size_t nb = (size_t)((s + 1) * 32 + h) * 512;
#pragma unroll
      for (int u = 0; u < 2; ++u)
        __builtin_amdgcn_global_load_lds((const GAS void*)(a.Kf + nb + (2 * w + u) * 64 + lane), (LAS void*)(L + L_KX + (2 * w + u) * 1024), 16, 0, 0);
#pragma unroll
      for (int T = 0; T < 2; ++T) VX[T] = a.Vf[nb + (2 * w + T) * 64 + lane];
    }
    for (unsigned spins = 0; !__all(fl[1] == tag); ++spins) {
      asm volatile("" ::: "memory");
      fl = __builtin_amdgcn_raw_buffer_load_b64(rs, WS_FLAGS + s * 4096 + (lane & 31) * 128, 0, 16);
      if (spins > SPIN * 16) { *err = 4; break; }
    }
    const unsigned tau = flag_max(fl[0], tp) + a.V1 + a.V2;
    while ((int)(RT() - tau) < 0) {}
#pragma unroll
    for (int u = 0; u < 2; ++u)
      __builtin_amdgcn_global_load_lds((const GAS void*)(a.ws + WS_MEMX + ((s + 1) & 3) * 65536 + msrc + (2 * w + u) * 1024), (LAS void*)(L + L_MEM + (2 * w + u) * 1024), 16, 0, 16);
  }
}

__global__ void __launch_bounds__(256) k_attn(const u32x4* Qf, const u32x4* Kf, const u32x4* Vf, const u32x4* KMf, const u32x2* VMf, unsigned short* O) {
  int item = blockIdx.x * 4 + (threadIdx.x >> 6), lane = threadIdx.x & 63;
  int s = item >> 5, hd = item & 31, c = lane & 15, g = lane >> 4;
  size_t base = (size_t)item * 512;
#pragma unroll
  for (int qt = 0; qt < 2; ++qt) {
    f32x4 s0 = {0.f, 0.f, 0.f, 0.f}, s1 = s0, s2 = s0;
#pragma unroll
    for (int kk = 0; kk < 4; ++kk) {
      u32x4 q = Qf[base + (qt * 4 + kk) * 64 + lane];
      s0 = mfma32(KMf[((size_t)item * 4 + kk) * 64 + lane], q, s0);
      s1 = mfma32(Kf[base + kk * 64 + lane], q, s1);
      s2 = mfma32(Kf[base + (4 + kk) * 64 + lane], q, s2);
    }
    int qi = qt * 16 + c;
    const float NEG = -3.0e38f;
#pragma unroll
    for (int j = 0; j < 4; ++j) {
      if (4 * g + j > qi) s1[j] = NEG;
      if (16 + 4 * g + j > qi) s2[j] = NEG;
    }
    float mx = fmaxf(fmaxf(fmaxf(s1[0], s1[1]), fmaxf(s1[2], s1[3])), fmaxf(fmaxf(s2[0], s2[1]), fmaxf(s2[2], s2[3])));
    mx = fmaxf(mx, fmaxf(s0[0], s0[1]));
    mx = xmax32(xmax16(mx));
    float p0[2], p1[4], p2[4];
#pragma unroll
    for (int j = 0; j < 2; ++j) p0[j] = __builtin_amdgcn_exp2f((s0[j] - mx) * SCL);
#pragma unroll
    for (int j = 0; j < 4; ++j) {
      p1[j] = (4 * g + j > qi) ? 0.f : __builtin_amdgcn_exp2f((s1[j] - mx) * SCL);
      p2[j] = (16 + 4 * g + j > qi) ? 0.f : __builtin_amdgcn_exp2f((s2[j] - mx) * SCL);
    }
    const u32x4 PX = {pk2(p1[0], p1[1]), pk2(p1[2], p1[3]), pk2(p2[0], p2[1]), pk2(p2[2], p2[3])};
    const u32x2 PM = {pk2(p0[0], p0[1]), 0u};
    float sum = (bflo(PX[0]) + bfhi(PX[0])) + (bflo(PX[1]) + bfhi(PX[1])) + (bflo(PX[2]) + bfhi(PX[2])) + (bflo(PX[3]) + bfhi(PX[3])) + (bflo(PM[0]) + bfhi(PM[0]));
    sum = xadd32(xadd16(sum));
    const float rinv = 1.0f / sum;
#pragma unroll
    for (int E = 0; E < 8; ++E) {
      f32x4 o = {0.f, 0.f, 0.f, 0.f};
      o = mfma32(Vf[base + E * 64 + lane], PX, o);
      u32x2 vm = VMf[((size_t)item * 4 + (E >> 1)) * 64 + lane];
      o = mfma16((u32x2){vm[E & 1], 0u}, PM, o);
      *(u32x2*)(O + (size_t)(s * 32 + qi) * 4096 + hd * 128 + E * 16 + 4 * g) = (u32x2){pk2(o[0] * rinv, o[1] * rinv), pk2(o[2] * rinv, o[3] * rinv)};
    }
  }
}

template <int MODE>
__global__ void __launch_bounds__(256) k_gemm(const unsigned short* A, int K, const u32x4* Wf, int N, const unsigned short* res,
                                              const float* gate, const unsigned short* bias, unsigned short* out) {
  int lane = threadIdx.x & 63, w = threadIdx.x >> 6, c = lane & 15, g = lane >> 4;
  int nb = N / 128;
  int rb = blockIdx.x / nb, cb = blockIdx.x % nb;
  int row0 = rb * 32, nt0 = cb * 8 + w * 2, nks = K / 32;
  f32x4 acc[2][2] = {};
  for (int ks = 0; ks < nks; ++ks) {
    u32x4 a[2], b[2];
#pragma unroll
    for (int mt = 0; mt < 2; ++mt) a[mt] = *(const u32x4*)(A + (size_t)(row0 + mt * 16 + c) * K + ks * 32 + g * 8);
#pragma unroll
    for (int T = 0; T < 2; ++T) b[T] = Wf[((size_t)(nt0 + T) * nks + ks) * 64 + lane];
#pragma unroll
    for (int T = 0; T < 2; ++T)
#pragma unroll
      for (int mt = 0; mt < 2; ++mt) acc[T][mt] = mfma32(b[T], a[mt], acc[T][mt]);
  }
#pragma unroll
  for (int T = 0; T < 2; ++T)
#pragma unroll
    for (int mt = 0; mt < 2; ++mt) {
      int row = row0 + mt * 16 + c, col = (nt0 + T) * 16 + 4 * g;
      float r[4];
#pragma unroll
      for (int j = 0; j < 4; ++j) {
        float v = rbf(acc[T][mt][j]);
        if (MODE == 0) r[j] = bf2f(res[(size_t)row * N + col + j]) + rbf(gate[col + j] * v);
        if (MODE == 1) {
          float x = rbf(v + bf2f(bias[col + j]));
          float u = 0.7978845608028654f * (x + 0.044715f * x * x * x);
          float t = 1.0f - 2.0f / (1.0f + __expf(2.0f * u));
          r[j] = 0.5f * x * (1.0f + t);
        }
        if (MODE == 2) r[j] = bf2f(res[(size_t)row * N + col + j]) + rbf(gate[col + j] * rbf(v + bf2f(bias[col + j])));
      }
      *(u32x2*)(out + (size_t)row * N + col) = (u32x2){pk2(r[0], r[1]), pk2(r[2], r[3])};
    }
}

#define CV1 15
#define CV2 62
#define CV3 55

extern "C" void kernel_launch(void* const* d_in, const int* in_sizes, int n_in, void* d_out, int out_size, void* d_ws,
                              size_t ws_size, hipStream_t stream) {
  typedef const unsigned short* cus;
  cus x = (cus)d_in[0], mem0 = (cus)d_in[1], Wq = (cus)d_in[2], Wk = (cus)d_in[3], Wv = (cus)d_in[4], Wo = (cus)d_in[5];
  cus w1 = (cus)d_in[8], b1 = (cus)d_in[9], w2 = (cus)d_in[10], b2 = (cus)d_in[11], cc = (cus)d_in[12], wmod = (cus)d_in[13];
  char* ws = (char*)d_ws;
  float* mod = (float*)(ws + WS_MOD);
  (void)hipMemsetAsync(ws + WS_CTL, 0, CTL_ZERO_BYTES, stream);
  (void)hipMemsetAsync(ws + WS_PART, 0xff, RING_BYTES, stream);
  k_prep_frag<<<1024, 256, 0, stream>>>(Wq, 512, 4096, (u32x4*)(ws + WS_WQF));
  k_prep_frag<<<1024, 256, 0, stream>>>(Wk, 512, 4096, (u32x4*)(ws + WS_WKF));
  k_prep_frag<<<1024, 256, 0, stream>>>(Wv, 512, 4096, (u32x4*)(ws + WS_WVF));
  k_prep_frag<<<1024, 256, 0, stream>>>(Wo, 4096, 512, (u32x4*)(ws + WS_WOF));
  k_prep_frag<<<448, 256, 0, stream>>>(w1, 512, 1792, (u32x4*)(ws + WS_W1F));
  k_prep_frag<<<448, 256, 0, stream>>>(w2, 1792, 512, (u32x4*)(ws + WS_W2F));
  k_prep_woc<<<1024, 256, 0, stream>>>(Wo, (u32x4*)(ws + WS_WOC));
  k_mod<<<48, 256, 0, stream>>>(cc, wmod, mod);
  k_ln<<<512, 256, 0, stream>>>(x, mod, mod + 512, (unsigned short*)(ws + WS_H));
  k_qkv<<<2048, 256, 0, stream>>>((cus)(ws + WS_H), (u32x4*)(ws + WS_WQF), (u32x4*)(ws + WS_WKF), (u32x4*)(ws + WS_WVF),
                                  (u32x4*)(ws + WS_QF), (u32x4*)(ws + WS_KF), (u32x4*)(ws + WS_VF));
  ChainArgs a;
  a.wq = (u32x4*)(ws + WS_WQF); a.wk = (u32x4*)(ws + WS_WKF); a.wv = (u32x4*)(ws + WS_WVF); a.woc = (u32x4*)(ws + WS_WOC);
  a.mem0 = mem0; a.Kf = (u32x4*)(ws + WS_KF); a.Vf = (u32x4*)(ws + WS_VF); a.KMf = (u32x4*)(ws + WS_KMF); a.VMf = (u32x2*)(ws + WS_VMF);
  a.ws = ws; a.V1 = CV1; a.V2 = CV2; a.V3 = CV3; a.pad = 0;
  (void)hipFuncSetAttribute((const void*)k_chain, hipFuncAttributeMaxDynamicSharedMemorySize, L_END);
  k_chain<<<64, 256, L_END, stream>>>(a);
  k_attn<<<512, 256, 0, stream>>>((u32x4*)(ws + WS_QF), (u32x4*)(ws + WS_KF), (u32x4*)(ws + WS_VF), (u32x4*)(ws + WS_KMF), (u32x2*)(ws + WS_VMF), (unsigned short*)(ws + WS_O));
  k_gemm<0><<<64 * 4, 256, 0, stream>>>((cus)(ws + WS_O), 4096, (u32x4*)(ws + WS_WOF), 512, x, mod + 1024, nullptr, (unsigned short*)(ws + WS_X1));
  k_ln<<<512, 256, 0, stream>>>((cus)(ws + WS_X1), mod + 1536, mod + 2048, (unsigned short*)(ws + WS_H2));
  k_gemm<1><<<64 * 14, 256, 0, stream>>>((cus)(ws + WS_H2), 512, (u32x4*)(ws + WS_W1F), 1792, nullptr, nullptr, b1, (unsigned short*)(ws + WS_G));
  k_gemm<2><<<64 * 4, 256, 0, stream>>>((cus)(ws + WS_G), 1792, (u32x4*)(ws + WS_W2F), 512, (cus)(ws + WS_X1), mod + 2560, b2, (unsigned short*)d_out);
}
```

```cpp
#include <hip/hip_runtime.h>
#include <hip/hip_bf16.h>
#include <stdint.h>
#define NOPF

typedef __bf16 bf16x2 __attribute__((ext_vector_type(2)));
typedef __bf16 bf16x8 __attribute__((ext_vector_type(8)));
typedef short s16x4 __attribute__((ext_vector_type(4)));
typedef float f32x2 __attribute__((ext_vector_type(2)));
typedef float f32x4 __attribute__((ext_vector_type(4)));
typedef unsigned u32x2 __attribute__((ext_vector_type(2)));
typedef unsigned u32x4 __attribute__((ext_vector_type(4)));
typedef unsigned u32x8 __attribute__((ext_vector_type(8)));
typedef __bf16 bf16x16 __attribute__((ext_vector_type(16)));
typedef const unsigned short* cus;
#define GAS __attribute__((address_space(1)))
#define LAS __attribute__((address_space(3)))
#define DEV __device__ __forceinline__
typedef __amdgpu_buffer_rsrc_t rsrc_t;

constexpr int DM = 512, NSEG = 64, FF = 1792;
constexpr unsigned MB = 1u << 20;
constexpr unsigned WS_EPOCH = 0, WS_ERR = 256 * 1024;
constexpr unsigned F_IN = 98304, F_CAL = 8192, F_H = 32768, F_RDY = 65536;
constexpr unsigned WS_ST = 2 * MB + 512 * 1024;
constexpr unsigned P_O = 0, P_KV = 65536, P_X1 = 131072, P_G = 147456, P_ARC = 262144;
constexpr unsigned WS_MOD = 196608, WS_DBG = 3 * MB + 65536;
constexpr unsigned WS_HF = 4 * MB, WS_X1F = 6 * MB, WS_GF = 10 * MB;
constexpr unsigned WS_OF = 32 * MB;
constexpr unsigned HP_K = 0, HP_V = 512 * 1024, HP_KMV = 1024 * 1024;
constexpr unsigned C_PART = 0, C_FLAG = 1 * MB, C_MEMX = 1 * MB + 512 * 1024;
constexpr unsigned SENT = 0xffffffffu;
constexpr int LDS_BYTES = 163840;
constexpr float SCL = 0.08838834764831845f * 1.4426950408889634f;

struct Params {
  cus x, mem0, Wq, Wk, Wv, Wo, w1, b1, w2, b2, c, wmod;
  unsigned short* out;
  char* ws;
  unsigned comm;
  int V1, V2, V3, EA, pad;
};

DEV float bf2f(unsigned short u) { return __uint_as_float((unsigned)u << 16); }
DEV float bflo(unsigned u) { return __uint_as_float(u << 16); }
DEV float bfhi(unsigned u) { return __uint_as_float(u & 0xffff0000u); }
DEV unsigned pk2(float a, float b) {
  f32x2 p = {a, b};
  return __builtin_bit_cast(unsigned, __builtin_convertvector(p, bf16x2));
}
DEV float rbf(float a) { return bflo(pk2(a, a)); }
DEV f32x4 mfma32(u32x4 a, u32x4 b, f32x4 c) {
  return __builtin_amdgcn_mfma_f32_16x16x32_bf16(__builtin_bit_cast(bf16x8, a), __builtin_bit_cast(bf16x8, b), c, 0, 0, 0);
}
DEV f32x4 smf(u32x4 a, u32x8 b, f32x4 c, int idx) {
  return __builtin_amdgcn_smfmac_f32_16x16x64_bf16(__builtin_bit_cast(bf16x8, a), __builtin_bit_cast(bf16x16, b), c, idx, 0, 0);
}
DEV u32x4 half8(u32x8 v, int hi) { return hi ? __builtin_shufflevector(v, v, 4, 5, 6, 7) : __builtin_shufflevector(v, v, 0, 1, 2, 3); }
DEV u32x4 pk8(f32x4 a, f32x4 b) { return (u32x4){pk2(a[0], a[1]), pk2(a[2], a[3]), pk2(b[0], b[1]), pk2(b[2], b[3])}; }
DEV float xmax16(float v) {
  auto r = __builtin_amdgcn_permlane16_swap(__float_as_uint(v), __float_as_uint(v), false, false);
  return fmaxf(__uint_as_float(r[0]), __uint_as_float(r[1]));
}
DEV float xmax32(float v) {
  auto r = __builtin_amdgcn_permlane32_swap(__float_as_uint(v), __float_as_uint(v), false, false);
  return fmaxf(__uint_as_float(r[0]), __uint_as_float(r[1]));
}
DEV float xadd16(float v) {
  auto r = __builtin_amdgcn_permlane16_swap(__float_as_uint(v), __float_as_uint(v), false, false);
  return __uint_as_float(r[0]) + __uint_as_float(r[1]);
}
DEV float xadd32(float v) {
  auto r = __builtin_amdgcn_permlane32_swap(__float_as_uint(v), __float_as_uint(v), false, false);
  return __uint_as_float(r[0]) + __uint_as_float(r[1]);
}
DEV int opaque_lane() {
  int l;
  asm volatile("v_mbcnt_lo_u32_b32 %0, -1, 0\n\tv_mbcnt_hi_u32_b32 %0, -1, %0" : "=v"(l));
  return l;
}
#define BAR() asm volatile("s_waitcnt lgkmcnt(0)\n\ts_barrier" ::: "memory")
#define SB() __builtin_amdgcn_sched_barrier(0)
#define DRAIN() asm volatile("s_waitcnt vmcnt(0)" ::: "memory")
DEV u32x4 ld16(rsrc_t rs, unsigned voff, unsigned soff) { return __builtin_amdgcn_raw_buffer_load_b128(rs, voff, soff, 16); }
DEV void st16(rsrc_t rs, u32x4 v, unsigned voff, unsigned soff) { __builtin_amdgcn_raw_buffer_store_b128(v, rs, voff, soff, 16); }
DEV u32x2 trd(LAS char* p) { return __builtin_bit_cast(u32x2, __builtin_amdgcn_ds_read_tr16_b64_v4i16((LAS s16x4*)p)); }

#define TS(k, slot, item, j)
DEV void dma4k(const char* ws, unsigned voff, unsigned soff, unsigned ldsoff) {
  unsigned keep;
  asm volatile(
      "s_mov_b32 %0, m0\n\ts_mov_b32 m0, %3\n\ts_nop 0\n\t"
      "global_load_lds_dwordx4 %1, %2 sc1\n\tglobal_load_lds_dwordx4 %1, %2 offset:1024 sc1\n\t"
      "global_load_lds_dwordx4 %1, %2 offset:2048 sc1\n\tglobal_load_lds_dwordx4 %1, %2 offset:3072 sc1\n\ts_mov_b32 m0, %0"
      : "=&s"(keep) : "v"(voff), "s"(ws + soff), "s"(ldsoff) : "memory");
}
struct Ctx {
  rsrc_t rs;
  u32x2 tag;
  LAS char* L;
  int lane, w;
  unsigned far;
  bool dead;
};
DEV u32x2 tag_load(const Ctx& k) { return __builtin_amdgcn_raw_buffer_load_b64(k.rs, 0, WS_EPOCH, 16); }
DEV void get_tag(Ctx& k, u32x2 e) {
  k.tag[0] = __builtin_amdgcn_readfirstlane(e[0]) + 0x9e3779b1u;
  k.tag[1] = __builtin_amdgcn_readfirstlane(e[1]) + 0x00010003u;
}
template <int N2>
DEV void mod_get(Ctx& k, unsigned voff, unsigned soff, float* out) {
  u32x4 v[N2];
#pragma unroll 1
  for (unsigned spins = 0; !k.dead; ++spins) {
    asm volatile("" ::: "memory");
    unsigned bad = 0;
#pragma unroll
    for (int i = 0; i < N2; ++i) v[i] = ld16(k.rs, voff + i * 16, WS_MOD + soff);
#pragma unroll
    for (int i = 0; i < N2; ++i) bad |= (v[i][1] ^ k.tag[0]) | (v[i][3] ^ k.tag[0]);
    if (__all(bad == 0)) break;
    if (spins > 100000) { k.dead = true; __builtin_amdgcn_raw_buffer_store_b32(0x500u, k.rs, 0, WS_ERR, 16); }
    __builtin_amdgcn_s_sleep(1);
  }
#pragma unroll
  for (int i = 0; i < N2; ++i) { out[2 * i] = __uint_as_float(v[i][0]); out[2 * i + 1] = __uint_as_float(v[i][2]); }
}
DEV void set_flag8(const Ctx& k, unsigned off) {
  if (k.lane == 0) __builtin_amdgcn_raw_buffer_store_b64(k.tag, k.rs, 0, off, 16);
}
DEV void set_flag(const Ctx& k, unsigned off) {
  if (k.lane < 8) st16(k.rs, (u32x4){k.tag[0], k.tag[1], k.tag[0], k.tag[1]}, k.lane * 16, off);
}
DEV void set_prog(const Ctx& k, unsigned off, int s) {
  if (k.lane < 8) st16(k.rs, (u32x4){k.tag[0] + s + 1, k.tag[1], k.tag[0] + s + 1, k.tag[1]}, k.lane * 16, k.far + off);
}
DEV bool prog_ok(const Ctx& k, u32x2 f, int s) { return f[0] - k.tag[0] - (unsigned)(s + 1) < 64u && f[1] == k.tag[1]; }
DEV void wait_prog(Ctx& k, unsigned off, int n, int s) {
  const unsigned vo = (k.lane < n ? k.lane : 0) * 128;
#pragma unroll 1
  for (unsigned spins = 0; !k.dead; ++spins) {
    asm volatile("" ::: "memory");
    u32x2 f = __builtin_amdgcn_raw_buffer_load_b64(k.rs, vo, k.far + off, 16);
    if (__all(prog_ok(k, f, s))) break;
    if (spins > 100000) {
      k.dead = true;
      __builtin_amdgcn_raw_buffer_store_b32(0x400u, k.rs, 0, WS_ERR, 16);
    }
    __builtin_amdgcn_s_sleep(1);
  }
  asm volatile("" ::: "memory");
}
DEV void wait_flags(Ctx& k, unsigned off, int n, unsigned stride) {
  const unsigned vo = (k.lane < n ? k.lane : 0) * stride;
#pragma unroll 1
  for (unsigned spins = 0; !k.dead; ++spins) {
    asm volatile("" ::: "memory");
    u32x2 f = __builtin_amdgcn_raw_buffer_load_b64(k.rs, vo, off, 16);
    if (__all(f[0] == k.tag[0] && f[1] == k.tag[1])) break;
    if (spins > 100000) {
      k.dead = true;
      __builtin_amdgcn_raw_buffer_store_b32(0x100u, k.rs, 0, WS_ERR, 16);
    }
    __builtin_amdgcn_s_sleep(2);
  }
  asm volatile("" ::: "memory");
}

template <int NK, bool PERM>
DEV void stage_w(const Ctx& k, cus W, int ld, LAS char* dst) {
  const int l = k.lane, g = l >> 4, q = (l >> 2) & 3;
  cus src = W + (size_t)(PERM ? 4 * g + q : 8 * g + q) * ld + 8 * (l & 3);
#pragma unroll
  for (int i = 0; i < 2 * NK; ++i) {
    const int ks = i >> 1, u = i & 1;
    __builtin_amdgcn_global_load_lds((const GAS void*)(src + (size_t)(32 * ks + (PERM ? 16 : 4) * u) * ld), (LAS void*)(dst + i * 1024), 16, 0, 0);
  }
}
template <int NK, bool ACC>
DEV void read_w(const Ctx& k, LAS char* src, u32x4 (*F)[2]) {
  LAS char* p = src + k.lane * 16;
#pragma unroll
  for (int ks = 0; ks < NK; ++ks)
#pragma unroll
    for (int T = 0; T < 2; ++T) {
      u32x2 a = trd(p + ks * 2048 + 8 * T), b = trd(p + ks * 2048 + 8 * T + 1024);
      F[ks][T] = (u32x4){a[0], a[1], b[0], b[1]};
      if (ACC) asm volatile("" : "+a"(F[ks][T]));
    }
}
DEV void load_mod2(Ctx& k, int si, int ci, LAS char* sc, LAS char* sh);
DEV void ln_tile(Ctx& k, const u32x4 (&xv)[4], LAS char* red, LAS char* sc, LAS char* sh, u32x4 (&out)[4], int late) {
  const int c = k.lane & 15, g = k.lane >> 4;
  float f[32];
#pragma unroll
  for (int i = 0; i < 4; ++i)
#pragma unroll
    for (int j = 0; j < 4; ++j) { f[8 * i + 2 * j] = bflo(xv[i][j]); f[8 * i + 2 * j + 1] = bfhi(xv[i][j]); }
  float s = 0.f;
#pragma unroll
  for (int j = 0; j < 32; ++j) s += f[j];
  s = xadd32(xadd16(s));
  const float ml = s * (1.0f / 128);
  float q = 0.f;
#pragma unroll
  for (int j = 0; j < 32; ++j) q = fmaf(f[j] - ml, f[j] - ml, q);
  q = xadd32(xadd16(q));
  if (g == 0) *(LAS f32x2*)(red + c * 32 + k.w * 8) = (f32x2){ml, q};
  if (late) __builtin_amdgcn_s_sleep(70);
  load_mod2(k, 0, 1, sc, sh);
  BAR();
  const f32x4 r0 = *(LAS f32x4*)(red + c * 32), r1 = *(LAS f32x4*)(red + c * 32 + 16);
  const float m = 0.25f * ((r0[0] + r0[2]) + (r1[0] + r1[2]));
  const float d0 = r0[0] - m, d1 = r0[2] - m, d2 = r1[0] - m, d3 = r1[2] - m;
  const float var = (((r0[1] + r0[3]) + (r1[1] + r1[3])) + 128.f * ((d0 * d0 + d1 * d1) + (d2 * d2 + d3 * d3))) * (1.0f / DM);
  const float mb = rbf(m), rs = __builtin_amdgcn_rcpf(rbf(__builtin_amdgcn_sqrtf(rbf(rbf(var) + 1e-5f))));
#pragma unroll
  for (int i = 0; i < 4; ++i) {
    const int o = ((4 * k.w + i) * 32 + 8 * g) * 4;
    const f32x4 a0 = *(LAS f32x4*)(sc + o), a1 = *(LAS f32x4*)(sc + o + 16), b0 = *(LAS f32x4*)(sh + o), b1 = *(LAS f32x4*)(sh + o + 16);
    f32x4 y0, y1;
#pragma unroll
    for (int j = 0; j < 4; ++j) {
      y0[j] = fmaf((f[8 * i + j] - mb) * rs, a0[j], b0[j]);
      y1[j] = fmaf((f[8 * i + 4 + j] - mb) * rs, a1[j], b1[j]);
    }
    out[i] = pk8(y0, y1);
  }
}
DEV void load_mod2(Ctx& k, int si, int ci, LAS char* sc, LAS char* sh) {
  float f[4];
  const int half = k.w & 1, isc = k.w < 2;
  mod_get<2>(k, k.lane * 32, (isc ? ci : si) * 4096 + half * 2048, f);
  if (isc) {
#pragma unroll
    for (int j = 0; j < 4; ++j) f[j] = rbf(1.0f + f[j]);
  }
  *(LAS f32x4*)((isc ? sc : sh) + half * 1024 + k.lane * 16) = (f32x4){f[0], f[1], f[2], f[3]};
}

DEV void task_mod_ln1(Ctx& k, const Params& p, int j) {
  const int tid = k.w * 64 + k.lane, c = k.lane & 15, g = k.lane >> 4;
  const u32x2 ep = tag_load(k);
  u32x4 xv[4];
#pragma unroll
  for (int i = 0; i < 4; ++i) xv[i] = *(const u32x4*)(p.x + (size_t)(j * 16 + c) * DM + (4 * k.w + i) * 32 + 8 * g);
  const unsigned warm = __builtin_amdgcn_raw_buffer_load_b32(k.rs, 0, WS_HF + j * 16384, 0);
  if (j >= 96) get_tag(k, ep);
  if (j < 96) {
    const int kg = tid >> 2, ch = tid & 3;
    u32x4 wv[8];
#pragma unroll
    for (int i = 0; i < 8; ++i) wv[i] = *(const u32x4*)(p.wmod + (size_t)(kg * 8 + i) * 3072 + j * 32 + ch * 8);
    const u32x4 cv = *(const u32x4*)(p.c + kg * 8);
    SB();
    get_tag(k, ep);
    float acc[8] = {};
#pragma unroll
    for (int i = 0; i < 8; ++i) {
      const float x = (i & 1) ? bfhi(cv[i >> 1]) : bflo(cv[i >> 1]);
      const float sx = rbf(x * __builtin_amdgcn_rcpf(1.0f + __builtin_amdgcn_exp2f(-1.4426950408889634f * x)));
#pragma unroll
      for (int e = 0; e < 4; ++e) { acc[2 * e] = fmaf(sx, bflo(wv[i][e]), acc[2 * e]); acc[2 * e + 1] = fmaf(sx, bfhi(wv[i][e]), acc[2 * e + 1]); }
    }
#pragma unroll
    for (int e = 0; e < 8; ++e) {
      float v = acc[e];
      v += __uint_as_float(__builtin_amdgcn_update_dpp(0, __float_as_uint(v), 0x124, 0xf, 0xf, false));
      v += __uint_as_float(__builtin_amdgcn_update_dpp(0, __float_as_uint(v), 0x128, 0xf, 0xf, false));
      acc[e] = xadd32(xadd16(v));
    }
    if (k.lane < 4) {
      *(LAS f32x4*)(k.L + 8192 + k.w * 128 + k.lane * 32) = (f32x4){acc[0], acc[1], acc[2], acc[3]};
      *(LAS f32x4*)(k.L + 8192 + k.w * 128 + k.lane * 32 + 16) = (f32x4){acc[4], acc[5], acc[6], acc[7]};
    }
    BAR();
    TS(k, 128 + j, 66, 1);
    if (k.w == 0 && k.lane < 32) {
      LAS float* r = (LAS float*)(k.L + 8192) + k.lane;
      __builtin_amdgcn_raw_buffer_store_b64((u32x2){__float_as_uint(rbf((r[0] + r[32]) + (r[64] + r[96]))), k.tag[0]}, k.rs, k.lane * 8, WS_MOD + j * 256, 16);
    }
  }
  asm volatile("" :: "v"(warm));
  TS(k, 128 + j, 64, 0);
  u32x4 hv[4];
  ln_tile(k, xv, k.L + 4096, k.L, k.L + 2048, hv, j >= 16);
  TS(k, 128 + j, 66, 2);
#pragma unroll
  for (int i = 0; i < 4; ++i) st16(k.rs, hv[i], k.lane * 16 + i * 1024, WS_HF + j * 16384 + k.w * 4096);
  DRAIN();
  BAR();
  if (k.w == 0) set_flag(k, F_H + j * 128);
  TS(k, 128 + j, 64, 2);
}

DEV void role_wo(Ctx& k, const Params& p, int j) {
  const int mt = j & 1, ns = (j >> 1) & 15, c = k.lane & 15, g = k.lane >> 4;
  u32x4 Wf[32][2];
  {
    cus W = p.Wo + (size_t)(k.w * 1024) * DM + ns * 32;
    LAS char* st = k.L + 16384 + k.w * 32768;
    stage_w<16, true>(k, W, DM, st);
    DRAIN();
    read_w<16, true>(k, st, Wf);
    asm volatile("s_waitcnt lgkmcnt(0)" ::: "memory");
    stage_w<16, true>(k, W + (size_t)512 * DM, DM, st);
    DRAIN();
    read_w<16, false>(k, st, Wf + 16);
  }
  TS(k, 128 + j, 64, 3);
  float gate[8];
  mod_get<4>(k, g * 64, 8192 + ns * 256, gate);
#pragma unroll 1
  for (int s = j >> 5; s < NSEG; s += 2) {
    u32x4 res;
    if (k.w == 0) res = *(const u32x4*)(p.x + (size_t)(s * 32 + mt * 16 + c) * DM + ns * 32 + 8 * g);
    wait_prog(k, P_O + ((s & 1) * 2 + mt) * 16384 + k.w * 2048, 16, s);
    TS(k, 128 + j, s, 0);
    const unsigned ab = WS_OF + (s * 2 + mt) * 131072 + k.w * 32768;
    u32x4 A[32];
#pragma unroll
    for (int i = 0; i < 32; ++i) A[i] = ld16(k.rs, k.lane * 16 + (i & 3) * 1024, ab + (i >> 2) * 4096);
    SB();
    f32x4 a0 = {0.f, 0.f, 0.f, 0.f}, a1 = a0;
#pragma unroll
    for (int i = 0; i < 32; ++i) { a0 = mfma32(Wf[i][0], A[i], a0); a1 = mfma32(Wf[i][1], A[i], a1); }
    TS(k, 128 + j, s, 1);
    LAS char* red = k.L + (s & 2) * 4096;
    if (k.w) {
      *(LAS f32x4*)(red + (k.w - 1) * 2048 + k.lane * 16) = a0;
      *(LAS f32x4*)(red + (k.w - 1) * 2048 + 1024 + k.lane * 16) = a1;
    }
    BAR();
    if (k.w == 0) {
#pragma unroll
      for (int i = 0; i < 3; ++i) { a0 += *(LAS f32x4*)(red + i * 2048 + k.lane * 16); a1 += *(LAS f32x4*)(red + i * 2048 + 1024 + k.lane * 16); }
      f32x4 y0, y1;
#pragma unroll
      for (int e = 0; e < 4; ++e) {
        y0[e] = ((e & 1) ? bfhi(res[e >> 1]) : bflo(res[e >> 1])) + rbf(gate[e] * rbf(a0[e]));
        y1[e] = ((e & 1) ? bfhi(res[2 + (e >> 1)]) : bflo(res[2 + (e >> 1)])) + rbf(gate[4 + e] * rbf(a1[e]));
      }
      const u32x4 yv = pk8(y0, y1);
      st16(k.rs, yv, k.lane * 16, WS_X1F + (s * 2 + mt) * 16384 + ns * 1024);
      float f[8], sm = 0.f, sq = 0.f;
#pragma unroll
      for (int e = 0; e < 4; ++e) { f[2 * e] = bflo(yv[e]); f[2 * e + 1] = bfhi(yv[e]); sm += f[2 * e] + f[2 * e + 1]; }
      sm = xadd32(xadd16(sm)) * (1.0f / 32);
#pragma unroll
      for (int e = 0; e < 8; ++e) sq = fmaf(f[e] - sm, f[e] - sm, sq);
      sq = xadd32(xadd16(sq));
      if (g == 0) __builtin_amdgcn_raw_buffer_store_b64((u32x2){__float_as_uint(sm), __float_as_uint(sq)}, k.rs, c * 8, WS_ST + ((s * 2 + mt) * 16 + ns) * 128, 16);
      DRAIN();
      set_prog(k, P_X1 + ((s & 1) * 32 + mt * 16 + ns) * 128, s);
      TS(k, 128 + j, s, 2);
    }
  }
}

DEV void role_down(Ctx& k, const Params& p, int j) {
  const int mt = j & 1, nq = j >> 2;
  u32x4 Wf[2][14][2];
  {
    cus W = p.w2 + (size_t)(k.w * 448) * DM + nq * 64;
    LAS char* st = k.L + 32768 + k.w * 32768;
    stage_w<14, false>(k, W, DM, st);
    DRAIN();
    read_w<14, true>(k, st, Wf[0]);
    asm volatile("s_waitcnt lgkmcnt(0)" ::: "memory");
    stage_w<14, false>(k, W + 32, DM, st);
    DRAIN();
    read_w<14, true>(k, st, Wf[1]);
  }
  TS(k, 192 + j, 64, 3);
  const int row = (k.w & 1) * 8 + (k.lane >> 3), ch = k.lane & 7;
  const int fl = ((ch & 3) * 16 + row) * 16;
  float gate[8], bias[8];
  {
    mod_get<4>(k, ch * 64, 20480 + nq * 512, gate);
    const u32x4 bv = *(const u32x4*)(p.b2 + nq * 64 + 8 * ch);
#pragma unroll
    for (int e = 0; e < 4; ++e) { bias[2 * e] = bflo(bv[e]); bias[2 * e + 1] = bfhi(bv[e]); }
  }
#pragma unroll 1
  for (int s = (j >> 1) & 1; s < NSEG; s += 2) {
    wait_prog(k, P_G + mt * 16384 + ((s & 1) * 32 + k.w * 7) * 128, 7, s);
    TS(k, 192 + j, s, 0);
    const unsigned ab = WS_GF + (s * 2 + mt) * 57344 + k.w * 14336;
    u32x4 A[14];
#pragma unroll
    for (int i = 0; i < 14; ++i) A[i] = ld16(k.rs, k.lane * 16 + (i & 3) * 1024, ab + (i >> 2) * 4096);
    const u32x4 res = ld16(k.rs, (ch >> 2) * 1024 + fl, WS_X1F + (s * 2 + mt) * 16384 + nq * 2048);
    SB();
    f32x4 acc[4] = {};
#pragma unroll
    for (int i = 0; i < 14; ++i)
#pragma unroll
      for (int t = 0; t < 4; ++t) acc[t] = mfma32(Wf[t >> 1][i][t & 1], A[i], acc[t]);
    TS(k, 192 + j, s, 1);
    LAS char* red = k.L + (s & 2) * 8192;
#pragma unroll
    for (int t = 0; t < 4; ++t) *(LAS f32x4*)(red + k.w * 4096 + t * 1024 + k.lane * 16) = acc[t];
    BAR();
    if (k.w < 2) {
      f32x4 a0 = {0.f, 0.f, 0.f, 0.f}, a1 = a0;
#pragma unroll
      for (int i = 0; i < 4; ++i) { a0 += *(LAS f32x4*)(red + i * 4096 + (ch >> 2) * 2048 + fl); a1 += *(LAS f32x4*)(red + i * 4096 + (ch >> 2) * 2048 + 1024 + fl); }
      f32x4 y0, y1;
#pragma unroll
      for (int e = 0; e < 4; ++e) {
        y0[e] = ((e & 1) ? bfhi(res[e >> 1]) : bflo(res[e >> 1])) + rbf(gate[e] * rbf(rbf(a0[e]) + bias[e]));
        y1[e] = ((e & 1) ? bfhi(res[2 + (e >> 1)]) : bflo(res[2 + (e >> 1)])) + rbf(gate[4 + e] * rbf(rbf(a1[e]) + bias[4 + e]));
      }
      *(u32x4*)(p.out + (size_t)(s * 32 + mt * 16 + row) * DM + nq * 64 + 8 * ch) = pk8(y0, y1);
      TS(k, 192 + j, s, 2);
    }
  }
}

DEV void role_up(Ctx& k, const Params& p, int j) {
  const int mt = j & 1, par = (j >> 1) & 1, nb = j >> 2, c = k.lane & 15, g = k.lane >> 4;
  u32x4 Wf[2][16][2];
  f32x4 U[4] = {}, V[4];
  load_mod2(k, 3, 4, k.L, k.L + 2048);
#pragma unroll
  for (int sl = 0; sl < 2; ++sl) {
    LAS char* st = k.L + 32768 + k.w * 32768;
    const int col = nb * 256 + k.w * 64 + sl * 32;
    stage_w<16, false>(k, p.w1 + col, FF, st);
    DRAIN();
    read_w<16, false>(k, st, Wf[sl]);
    BAR();
    const u32x4 bv = *(const u32x4*)(p.b1 + col + 8 * g);
    f32x4 vb[2] = {};
    const u32x4 ones = {0x3f803f80u, 0x3f803f80u, 0x3f803f80u, 0x3f803f80u};
#pragma unroll
    for (int i = 0; i < 16; ++i) {
      const int o = (i * 32 + 8 * g) * 4;
      const f32x4 a0 = *(LAS f32x4*)(k.L + o), a1 = *(LAS f32x4*)(k.L + o + 16);
      const u32x4 sb = pk8(*(LAS f32x4*)(k.L + 2048 + o), *(LAS f32x4*)(k.L + 2048 + o + 16));
#pragma unroll
      for (int T = 0; T < 2; ++T) {
        u32x4 wv = Wf[sl][i][T];
        vb[T] = mfma32(wv, sb, vb[T]);
        wv = (u32x4){pk2(bflo(wv[0]) * a0[0], bfhi(wv[0]) * a0[1]), pk2(bflo(wv[1]) * a0[2], bfhi(wv[1]) * a0[3]),
                     pk2(bflo(wv[2]) * a1[0], bfhi(wv[2]) * a1[1]), pk2(bflo(wv[3]) * a1[2], bfhi(wv[3]) * a1[3])};
        U[2 * sl + T] = mfma32(wv, ones, U[2 * sl + T]);
        if (sl == 0 || i < 10) asm volatile("" : "+a"(wv));
        Wf[sl][i][T] = wv;
      }
    }
    V[2 * sl] = vb[0] + (f32x4){bflo(bv[0]), bfhi(bv[0]), bflo(bv[1]), bfhi(bv[1])};
    V[2 * sl + 1] = vb[1] + (f32x4){bflo(bv[2]), bfhi(bv[2]), bflo(bv[3]), bfhi(bv[3])};
  }
  TS(k, 224 + j, 64, 3);
  BAR();
#pragma unroll 1
  for (int s = par; s < NSEG; s += 2) {
    wait_prog(k, P_X1 + (par * 32 + mt * 16) * 128, 16, s);
    TS(k, 224 + j, s, 0);
    const int bo = (s & 2) * 8192;
    dma4k(p.ws, k.lane * 16, WS_X1F + (s * 2 + mt) * 16384 + k.w * 4096, bo + k.w * 4096);
    u32x2 st[4];
#pragma unroll
    for (int i = 0; i < 4; ++i) st[i] = __builtin_amdgcn_raw_buffer_load_b64(k.rs, (g * 4 + i) * 128 + c * 8, WS_ST + (s * 2 + mt) * 2048, 16);
    DRAIN();
    BAR();
    u32x4 A[16];
#pragma unroll
    for (int i = 0; i < 16; ++i) A[i] = *(LAS u32x4*)(k.L + bo + i * 1024 + k.lane * 16);
    SB();
    f32x4 acc[4] = {}, y[4];
#pragma unroll
    for (int i = 0; i < 16; ++i) acc[0] = mfma32(Wf[0][i][0], A[i], acc[0]);
    float m = (__uint_as_float(st[0][0]) + __uint_as_float(st[1][0])) + (__uint_as_float(st[2][0]) + __uint_as_float(st[3][0]));
    m = xadd32(xadd16(m)) * (1.0f / 16);
    float q = 0.f;
#pragma unroll
    for (int i = 0; i < 4; ++i) { const float d = __uint_as_float(st[i][0]) - m; q += fmaf(32.f * d, d, __uint_as_float(st[i][1])); }
    q = xadd32(xadd16(q)) * (1.0f / DM);
    const float mb = rbf(m), rs = __builtin_amdgcn_rcpf(rbf(__builtin_amdgcn_sqrtf(rbf(rbf(q) + 1e-5f))));
    for (int i = 0; i < 16; ++i) { __builtin_amdgcn_sched_group_barrier(0x8, 1, 0); __builtin_amdgcn_sched_group_barrier(0x402, 3, 0); }
    SB();
    const auto act = [&](int t) {
#pragma unroll
      for (int e = 0; e < 4; ++e) {
        const float x = rbf(fmaf(fmaf(-mb, U[t][e], acc[t][e]), rs, V[t][e]));
        float x2 = x * x;
        asm volatile("" : "+v"(x2));
        const float u = x * fmaf(x2, 0.044715f * 2.302208198f, 2.302208198f);
        float r = fmaf(-x, __builtin_amdgcn_rcpf(1.0f + __builtin_amdgcn_exp2f(u)), x);
        asm volatile("" : "+v"(r));
        y[t][e] = r;
      }
    };
    const unsigned go = WS_GF + (s * 2 + mt) * 57344 + (nb * 8 + k.w * 2) * 1024;
#pragma unroll
    for (int t = 1; t < 4; ++t) {
#pragma unroll
      for (int i = 0; i < 16; ++i) acc[t] = mfma32(Wf[t >> 1][i][t & 1], A[i], acc[t]);
      act(t - 1);
      __builtin_amdgcn_sched_group_barrier(0x8, 3, 1);
      for (int i = 0; i < 13; ++i) { __builtin_amdgcn_sched_group_barrier(0x8, 1, 1); __builtin_amdgcn_sched_group_barrier(0x402, 4, 1); }
      SB();
      if (t == 2) st16(k.rs, pk8(y[0], y[1]), k.lane * 16, go);
    }
    act(3);
    st16(k.rs, pk8(y[2], y[3]), k.lane * 16 + 1024, go);
    DRAIN();
    set_prog(k, P_G + mt * 16384 + (par * 32 + nb * 4 + k.w) * 128, s);
    TS(k, 224 + j, s, 2);
  }
}

#define CLOB_V "v12","v13","v16","v17","v18","v19","v20","v21","v22","v23","v24","v25","v26","v27","v28","v29","v30","v31","v32","v33","v34","v35","v36","v37","v38","v39","v40","v41","v42","v43","v44","v45","v46","v47","v48","v49","v50","v51","v52","v53","v54","v55","v56","v57","v58","v59","v60","v61","v62","v63","v64","v65","v66","v67","v68","v69","v70","v71","v72","v73","v74","v75","v76","v77","v78","v79","v80","v81","v82","v83","v84","v85","v86","v87","v88","v89","v90","v91","v92","v93","v94","v95","v96","v97","v98","v99","v100","v101","v102","v103","v104","v105","v106","v107","v108","v109","v110","v111","v112","v113","v114","v115","v116","v117","v118","v119","v120","v121","v122","v123","v124","v125","v126","v127","v128","v129","v130","v131","v132","v133","v134","v135","v136","v137","v138","v139","v140","v141","v142","v143","v144","v145","v146","v147","v148","v149","v150","v151","v152","v153","v154","v155","v156","v157","v158","v159","v160","v161","v162","v163","v164","v165","v166","v167","v168","v169","v170","v171","v172","v173","v174","v175","v176","v177","v178","v179","v180","v181","v182","v183","v184","v185","v186","v187","v188","v189","v190","v191","v192","v193","v194","v195","v196","v197","v198","v199","v200","v201","v202","v203","v204","v205","v206","v207","v208","v209","v210","v211","v212","v213","v214","v215","v216","v217","v218","v219","v220","v221","v222","v223","v224","v225","v226","v227","v228","v229","v230","v231","v232","v233","v234","v235","v236","v237","v238","v239","v240","v241","v242","v243","v244","v245","v246","v247","v248","v249","v250","v251","v254","v255"
#define CLOB_A "a0","a1","a2","a3","a4","a5","a6","a7","a8","a9","a10","a11","a12","a13","a14","a15","a16","a17","a18","a19","a20","a21","a22","a23","a24","a25","a26","a27","a28","a29","a30","a31","a32","a33","a34","a35","a36","a37","a38","a39","a40","a41","a42","a43","a44","a45","a46","a47","a48","a49","a50","a51","a52","a53","a54","a55","a56","a57","a58","a59","a60","a61","a62","a63","a64","a65","a66","a67","a68","a69","a70","a71","a72","a73","a74","a75","a76","a77","a78","a79","a80","a81","a82","a83","a84","a85","a86","a87","a88","a89","a90","a91","a92","a93","a94","a95","a96","a97","a98","a99","a100","a101","a102","a103","a104","a105","a106","a107","a108","a109","a110","a111","a112","a113","a114","a115","a116","a117","a118","a119","a120","a121","a122","a123","a124","a125","a126","a127","a128","a129","a130","a131","a132","a133","a134","a135","a136","a137","a138","a139","a140","a141","a142","a143","a144","a145","a146","a147","a148","a149","a150","a151","a152","a153","a154","a155","a156","a157","a158","a159","a160","a161","a162","a163","a164","a165","a166","a167","a168","a169","a170","a171","a172","a173","a174","a175","a176","a177","a178","a179","a180","a181","a182","a183","a184","a185","a186","a187","a188","a189","a190","a191","a192","a193","a194","a195","a196","a197","a198","a199","a200","a201","a202","a203","a204","a205","a206","a207","a208","a209","a210","a211","a212","a213","a214","a215","a216","a217","a218","a219","a220","a221","a222","a223","a224","a225","a226","a227","a228","a229","a230","a231","a232","a233","a234","a235","a236","a237","a238","a239","a240","a241","a242","a243","a244","a245","a246","a247","a248","a249","a250","a251","a252","a253","a254","a255"
#define CLOB_S "s40","s41","s42","s43","s44","s45","s46","s47","s48","s49","s50","s51","s52","s53","s54","s55","s56","s57","s58","s59","s60","s61","s62","s63","s64","s65","s66","s67","s68","s69","s70","s71","s72","s73","s74","s75","s76","s77","s78","s79","s80","s81","s82","s83","s84","s85","s86","s87","s88","s89","s90","s91","s92","s93","s94","s95","s96","s97","s98","s99"
#define CLOB_R "v8","v9","v10","v11","v12","v13","v14","v15","v16","v17","v18","v19","v20","v21","v22","v23","v24","v25","v26","v27","v28","v29","v30","v31","v32","v33","v34","v35","v36","v37","v38","v39","v40","v41","v42","v43","v45"

constexpr int L_MEM = 0, L_KX = 8192, L_Q = 24576, L_KM = 16384, L_O = 30720, L_WO = 32768;

DEV unsigned wait_cal(Ctx& k, unsigned comm, unsigned flip) {
#pragma unroll 1
  for (unsigned spins = 0; spins < 200000; ++spins) {
    asm volatile("" ::: "memory");
    const u32x2 f = __builtin_amdgcn_raw_buffer_load_b64(k.rs, 0, F_CAL, 16);
    const unsigned f0 = __builtin_amdgcn_readfirstlane(f[0]), f1 = __builtin_amdgcn_readfirstlane(f[1]) ^ k.tag[1];
    if (f0 == k.tag[0] && (f1 & 0x7fffffffu) == 0) return comm + (((f1 >> 31) ^ flip) << 13);
    __builtin_amdgcn_s_sleep(2);
  }
  return comm;
}
DEV int wmax32(int d) {
  d = max(d, __builtin_amdgcn_update_dpp(d, d, 0x121, 0xf, 0xf, false));
  d = max(d, __builtin_amdgcn_update_dpp(d, d, 0x122, 0xf, 0xf, false));
  d = max(d, __builtin_amdgcn_update_dpp(d, d, 0x124, 0xf, 0xf, false));
  d = max(d, __builtin_amdgcn_update_dpp(d, d, 0x128, 0xf, 0xf, false));
  return max(__builtin_amdgcn_readlane(d, 0), __builtin_amdgcn_readlane(d, 16));
}
#define AW 56
#define RT() ((unsigned)__builtin_amdgcn_s_memrealtime())
DEV void role_ctl(Ctx& k, const Params& p) {
  const unsigned lb = RT() - 3000, cb = wait_cal(k, p.comm, 0) + C_FLAG;
  wait_flags(k, F_RDY, 64, 128);
  volatile LAS unsigned* gate = (volatile LAS unsigned*)(k.L + 12288);
  if (k.w == 1) {
#pragma unroll 1
    for (int s = 0; s < NSEG - 1; ++s) {
      if (s && s + 2 < NSEG) {
        wait_prog(k, P_KV + (s & 1) * 32768, 64, s + 2);
        wait_prog(k, P_KV + (s & 1) * 32768 + 16384, 64, s + 2);
      }
      set_flag8(k, cb + (s >> 2) * 16384 + (s & 3) * 2048 + 1160);
      *gate = k.tag[0];
    }
    return;
  }
  int dhat = 120, dprev = 120;
  unsigned t1 = 0;
#pragma unroll 1
  for (int s = 0; s < NSEG - 1; ++s) {
    const unsigned blk = cb + (s >> 2) * 16384 + (s & 3) * 2048;
    const int sp = s ? s - 1 : 0;
    const unsigned pb = cb + (sp >> 2) * 16384 + (sp & 3) * 2048;
    if (s) while ((int)(RT() - t1) < 0) __builtin_amdgcn_s_sleep(1);
    u32x2 st;
#pragma unroll 1
    for (unsigned spins = 0; spins < 100000; ++spins) {
      asm volatile("" ::: "memory");
      st = __builtin_amdgcn_raw_buffer_load_b64(k.rs, k.lane * 32, pb, 16);
      if (!(unsigned)__ballot(st[0] - lb >= 0x80000u)) break;
      __builtin_amdgcn_s_sleep(3);
    }
    if (s == 0) {
      for (unsigned spins = 0; *gate != k.tag[0] && spins < 4000000; ++spins) {}
      t1 = RT() + 50;
    }
    else {
      const unsigned w1 = __builtin_amdgcn_readlane(st[0], 32);
      if ((int)(w1 - t1) > 0 && w1 - lb < 0x80000u) t1 = w1;
      const unsigned tp = lb + wmax32((int)(st[0] - lb));
      if ((int)(t1 - tp) < p.V3) t1 = tp + 45;
      if (s > 3) { const int d = min(wmax32((int)st[1]), dhat + 3); dhat = max(d, dprev); dprev = d; }
      t1 += p.V2 - p.EA + dhat + p.V1 + (s <= 2 ? 22 : 0);
    }
    if (k.lane < 2) __hip_atomic_fetch_max((GAS unsigned*)(p.ws + blk + 1024 + k.lane * 128), t1, __ATOMIC_RELAXED, __HIP_MEMORY_SCOPE_AGENT);
    if (k.lane < 8) st16(k.rs, (u32x4){t1 + AW, k.tag[0], t1 + AW, k.tag[0]}, k.lane * 16, k.far + 196608 + s * 128);
  }
}

typedef const __attribute__((address_space(4))) Params* kargp;
DEV Params fresh_params(unsigned comm) {
  kargp q = (kargp)__builtin_amdgcn_kernarg_segment_ptr();
  asm volatile("" : "+s"(q));
  Params r;
  r.x = q->x; r.mem0 = q->mem0; r.Wq = q->Wq; r.Wk = q->Wk; r.Wv = q->Wv; r.Wo = q->Wo; r.w1 = q->w1; r.b1 = q->b1; r.w2 = q->w2; r.b2 = q->b2;
  r.c = q->c; r.wmod = q->wmod; r.out = q->out; r.ws = q->ws; r.comm = comm; r.V1 = q->V1; r.V2 = q->V2; r.V3 = q->V3; r.EA = q->EA; r.pad = 0;
  return r;
}

__global__ void __launch_bounds__(256, 1) k_fused(Params p0) {
  extern __shared__ __attribute__((aligned(16))) char lds[];
  const int w = __builtin_amdgcn_readfirstlane(threadIdx.x >> 6);
  const int bid = blockIdx.x, xc = bid & 7;
  int idx = (bid >> 3) * 4 + (bid & 3);
  const unsigned comm = (unsigned)((((uintptr_t)p0.ws + 130 * MB - 1) & ~(uintptr_t)(2 * MB - 1)) - (uintptr_t)p0.ws);
  Ctx k;
  k.rs = __builtin_amdgcn_make_buffer_rsrc(p0.ws, 0, 256 * MB, 0x00020000);
  k.L = (LAS char*)lds;
  k.w = w;
  k.dead = false;
#define MAP 5
  int slot = (xc >= 4 ? 128 : 0) + idx;
  if (MAP == 1 || MAP == 2) { if (slot >= 64 && slot < 128) slot += 128; else if (slot >= 192) slot -= 128; }
  if (MAP == 3) { slot = xc < 2 ? (bid >> 3) * 2 + xc : 64 + (bid >> 3) * 6 + (xc - 2); idx = slot; }
  if (MAP == 5) {
    const int row = bid >> 3;
    slot = xc < 2 ? row * 2 + xc : (xc >= 6 ? 128 + row * 2 + (xc - 6) : (row < 16 ? 64 : 128) + row * 4 + (xc - 2));
    idx = slot;
  }
  if (MAP == 4) { slot = xc < 2 ? (bid >> 3) * 2 + xc : (xc < 4 ? 192 + (bid >> 3) * 2 + (xc - 2) : 64 + idx); idx = slot; }
  if (slot >= 128) {
    k.lane = opaque_lane();
    const Params p = fresh_params(comm);
    const int j = slot - 128;
    task_mod_ln1(k, p, j);
    if (w == 3) set_flag(k, F_IN + bid * 128);
    k.far = wait_cal(k, comm, 1);
    if (j < 124 && (MAP != 5 || j >= 64)) wait_prog(k, P_KV + (j & 63) * 128, 1, 0);
    if (MAP == 2 && j >= 64 && j < 124) return;
    if (j < 64) role_wo(k, p, j);
    else if (j < 96) role_down(k, p, j - 64);
    else if (j < 124) role_up(k, p, j - 96);
    else if (j == 125 && w < 2) {
      role_ctl(k, p);
    }
    else if (j == 124 && w == 0) {
#pragma unroll 1
      for (int i = 0; i < 4; ++i) wait_flags(k, F_IN + i * 8192, 64, 128);
      set_flag8(k, WS_EPOCH);
    }
    return;
  }
  if (slot >= 64) {
    const int lane = opaque_lane(), c = lane & 15, g = lane >> 4;
    const Params p = fresh_params(comm);
    const int si = slot - 64;
    const int h = MAP != 3 ? si & 31 : (si < 60 ? (si / 12) * 6 + si % 6 : 30 + ((si - 60) >> 1)), par = MAP != 3 ? si >> 5 : (si < 60 ? (si / 6) & 1 : si & 1);
    constexpr int SH_KM = 131072, SH_VM = 135168, SH_MEM = 137216;
    *(LAS u32x4*)(k.L + SH_KM + w * 1024 + lane * 16) = (u32x4){0, 0, 0, 0};
    const int tc = 2 * (c >> 2) + (c & 1), bc = (c >> 1) & 1;
    const int t = 2 * (c & 3) + (c >> 3), c1 = (c >> 2) & 1, q = (lane >> 2) & 3, pp = lane & 3;
    const unsigned scat = SH_MEM + 2 * w * 1024 + (4 * (t >> 1) + 2 * (g & 1) + (t & 1)) * 16 + (2 * (g >> 1) + c1) * 2;
    const unsigned src = (8 * w + g) * 8192 + (c ^ (2 * (g + 4 * (w & 1)))) * 16;
    const unsigned rd = g * 1024 + q * 256 + (((4 * w + (pp >> 1)) ^ (2 * (q + 4 * (g & 1)))) * 16) + (pp & 1) * 8;
    asm volatile(
    "s_mov_b64 s[40:41], %0\n"
    "s_mov_b32 s42, 0x10000000\n"
    "s_mov_b32 s43, 0x00020000\n"
    "s_mov_b32 s61, %1\n"
    "s_mov_b32 s60, %2\n"
    "s_mov_b32 s59, %3\n"
    "s_mov_b32 s71, %5\n"
    "s_mov_b32 s74, 0x400000\n"
    "s_mov_b32 s75, 0x00020000\n"
    "s_lshl_b32 s47, s61, 8\n"
    "s_lshl_b32 s62, s60, 10\n"
    "v_xor_b32 v11, 32, v9\n"
    "buffer_load_dwordx2 v[56:57], off, s[40:43], 0 sc1\n"
    "buffer_load_dword v59, off, s[40:43], s71\n"
    "buffer_load_dword v59, off, s[40:43], %9\n"
    "s_mov_b32 s52, 0x400000\n"
    "buffer_load_dword v59, off, s[40:43], s52\n"
    "s_mov_b64 s[76:77], %4\n"
    "s_mov_b32 s78, 8192\n"
    "s_mov_b32 s79, 0x00020000\n"
    "buffer_load_dword v60, v15, s[76:79], 0 offen offset:0\n"
    "buffer_load_dword v61, v15, s[76:79], 0 offen offset:8\n"
    "buffer_load_dword v62, v15, s[76:79], 0 offen offset:16\n"
    "buffer_load_dword v63, v15, s[76:79], 0 offen offset:24\n"
    "buffer_load_dword v64, v15, s[76:79], 0 offen offset:128\n"
    "buffer_load_dword v65, v15, s[76:79], 0 offen offset:136\n"
    "buffer_load_dword v66, v15, s[76:79], 0 offen offset:144\n"
    "buffer_load_dword v67, v15, s[76:79], 0 offen offset:152\n"
    "s_mov_b64 s[72:73], %7\n"
    "s_add_u32 m0, s62, 0\n"
    "s_add_u32 s52, s47, 0x0\n"
    "buffer_load_dwordx4 v8, s[72:75], s52 offen lds\n"
    "s_add_u32 m0, m0, 4096\n"
    "s_add_u32 s52, s47, 0x8000\n"
    "buffer_load_dwordx4 v8, s[72:75], s52 offen lds\n"
    "s_add_u32 m0, m0, 4096\n"
    "s_add_u32 s52, s47, 0x40000\n"
    "buffer_load_dwordx4 v8, s[72:75], s52 offen lds\n"
    "s_add_u32 m0, m0, 4096\n"
    "s_add_u32 s52, s47, 0x48000\n"
    "buffer_load_dwordx4 v8, s[72:75], s52 offen lds\n"
    "s_add_u32 m0, m0, 4096\n"
    "s_add_u32 s52, s47, 0x80000\n"
    "buffer_load_dwordx4 v8, s[72:75], s52 offen lds\n"
    "s_add_u32 m0, m0, 4096\n"
    "s_add_u32 s52, s47, 0x88000\n"
    "buffer_load_dwordx4 v8, s[72:75], s52 offen lds\n"
    "s_add_u32 m0, m0, 4096\n"
    "s_add_u32 s52, s47, 0xc0000\n"
    "buffer_load_dwordx4 v8, s[72:75], s52 offen lds\n"
    "s_add_u32 m0, m0, 4096\n"
    "s_add_u32 s52, s47, 0xc8000\n"
    "buffer_load_dwordx4 v8, s[72:75], s52 offen lds\n"
    "s_add_u32 m0, m0, 4096\n"
    "s_add_u32 s52, s47, 0x100000\n"
    "buffer_load_dwordx4 v8, s[72:75], s52 offen lds\n"
    "s_add_u32 m0, m0, 4096\n"
    "s_add_u32 s52, s47, 0x108000\n"
    "buffer_load_dwordx4 v8, s[72:75], s52 offen lds\n"
    "s_add_u32 m0, m0, 4096\n"
    "s_add_u32 s52, s47, 0x140000\n"
    "buffer_load_dwordx4 v8, s[72:75], s52 offen lds\n"
    "s_add_u32 m0, m0, 4096\n"
    "s_add_u32 s52, s47, 0x148000\n"
    "buffer_load_dwordx4 v8, s[72:75], s52 offen lds\n"
    "s_add_u32 m0, m0, 4096\n"
    "s_add_u32 s52, s47, 0x180000\n"
    "buffer_load_dwordx4 v8, s[72:75], s52 offen lds\n"
    "s_add_u32 m0, m0, 4096\n"
    "s_add_u32 s52, s47, 0x188000\n"
    "buffer_load_dwordx4 v8, s[72:75], s52 offen lds\n"
    "s_add_u32 m0, m0, 4096\n"
    "s_add_u32 s52, s47, 0x1c0000\n"
    "buffer_load_dwordx4 v8, s[72:75], s52 offen lds\n"
    "s_add_u32 m0, m0, 4096\n"
    "s_add_u32 s52, s47, 0x1c8000\n"
    "buffer_load_dwordx4 v8, s[72:75], s52 offen lds\n"
    "s_mov_b64 s[72:73], %7\n"
    "s_add_u32 m0, s62, 65536\n"
    "s_add_u32 s52, s47, 0x200000\n"
    "buffer_load_dwordx4 v8, s[72:75], s52 offen lds\n"
    "s_add_u32 m0, m0, 4096\n"
    "s_add_u32 s52, s47, 0x208000\n"
    "buffer_load_dwordx4 v8, s[72:75], s52 offen lds\n"
    "s_add_u32 m0, m0, 4096\n"
    "s_add_u32 s52, s47, 0x240000\n"
    "buffer_load_dwordx4 v8, s[72:75], s52 offen lds\n"
    "s_add_u32 m0, m0, 4096\n"
    "s_add_u32 s52, s47, 0x248000\n"
    "buffer_load_dwordx4 v8, s[72:75], s52 offen lds\n"
    "s_add_u32 m0, m0, 4096\n"
    "s_add_u32 s52, s47, 0x280000\n"
    "buffer_load_dwordx4 v8, s[72:75], s52 offen lds\n"
    "s_add_u32 m0, m0, 4096\n"
    "s_add_u32 s52, s47, 0x288000\n"
    "buffer_load_dwordx4 v8, s[72:75], s52 offen lds\n"
    "s_add_u32 m0, m0, 4096\n"
    "s_add_u32 s52, s47, 0x2c0000\n"
    "buffer_load_dwordx4 v8, s[72:75], s52 offen lds\n"
    "s_add_u32 m0, m0, 4096\n"
    "s_add_u32 s52, s47, 0x2c8000\n"
    "buffer_load_dwordx4 v8, s[72:75], s52 offen lds\n"
    "s_add_u32 m0, m0, 4096\n"
    "s_add_u32 s52, s47, 0x300000\n"
    "buffer_load_dwordx4 v8, s[72:75], s52 offen lds\n"
    "s_add_u32 m0, m0, 4096\n"
    "s_add_u32 s52, s47, 0x308000\n"
    "buffer_load_dwordx4 v8, s[72:75], s52 offen lds\n"
    "s_add_u32 m0, m0, 4096\n"
    "s_add_u32 s52, s47, 0x340000\n"
    "buffer_load_dwordx4 v8, s[72:75], s52 offen lds\n"
    "s_add_u32 m0, m0, 4096\n"
    "s_add_u32 s52, s47, 0x348000\n"
    "buffer_load_dwordx4 v8, s[72:75], s52 offen lds\n"
    "s_add_u32 m0, m0, 4096\n"
    "s_add_u32 s52, s47, 0x380000\n"
    "buffer_load_dwordx4 v8, s[72:75], s52 offen lds\n"
    "s_add_u32 m0, m0, 4096\n"
    "s_add_u32 s52, s47, 0x388000\n"
    "buffer_load_dwordx4 v8, s[72:75], s52 offen lds\n"
    "s_add_u32 m0, m0, 4096\n"
    "s_add_u32 s52, s47, 0x3c0000\n"
    "buffer_load_dwordx4 v8, s[72:75], s52 offen lds\n"
    "s_add_u32 m0, m0, 4096\n"
    "s_add_u32 s52, s47, 0x3c8000\n"
    "buffer_load_dwordx4 v8, s[72:75], s52 offen lds\n"
    "s_and_b32 s63, s60, 1\n"
    "s_lshr_b32 s64, s60, 1\n"
    "s_lshl_b32 s65, s60, 13\n"
    "s_mov_b32 s44, 0\n"
    "s_mov_b32 s45, 0\n"
    "s_mov_b32 s46, 0\n"
    "s_mov_b32 s48, 0\n"
    "s_lshl_b32 s52, s59, 8\n"
    "s_add_u32 s53, s52, 32768\n"
    "s_waitcnt vmcnt(16)\n"
    "v_readfirstlane_b32 s98, v56\n"
    "v_readfirstlane_b32 s99, v57\n"
    "s_nop 1\n"
    "s_add_u32 s98, s98, 0x9e3779b1\n"
    "s_add_u32 s99, s99, 0x00010003\n"
    "s_lshl_b32 s52, s60, 11\n"
    "s_add_u32 s52, s52, 137216\n"
    "v_add_u32 v58, s52, v0\n"
    "ds_write_b128 v58, v[60:63]\n"
    "ds_write_b128 v58, v[64:67] offset:1024\n"
    "s_barrier\n"
    "ds_read_b64_tr_b16 a[0:1], v9 offset:0\n"
    "ds_read_b64_tr_b16 a[64:65], v11 offset:0\n"
    "ds_read_b64_tr_b16 a[2:3], v9 offset:4096\n"
    "ds_read_b64_tr_b16 a[66:67], v11 offset:4096\n"
    "ds_read_b64_tr_b16 a[4:5], v9 offset:8192\n"
    "ds_read_b64_tr_b16 a[68:69], v11 offset:8192\n"
    "ds_read_b64_tr_b16 a[6:7], v9 offset:12288\n"
    "ds_read_b64_tr_b16 a[70:71], v11 offset:12288\n"
    "ds_read_b64_tr_b16 a[8:9], v9 offset:16384\n"
    "ds_read_b64_tr_b16 a[72:73], v11 offset:16384\n"
    "ds_read_b64_tr_b16 a[10:11], v9 offset:20480\n"
    "ds_read_b64_tr_b16 a[74:75], v11 offset:20480\n"
    "ds_read_b64_tr_b16 a[12:13], v9 offset:24576\n"
    "ds_read_b64_tr_b16 a[76:77], v11 offset:24576\n"
    "ds_read_b64_tr_b16 a[14:15], v9 offset:28672\n"
    "ds_read_b64_tr_b16 a[78:79], v11 offset:28672\n"
    "ds_read_b64_tr_b16 a[16:17], v9 offset:32768\n"
    "ds_read_b64_tr_b16 a[80:81], v11 offset:32768\n"
    "ds_read_b64_tr_b16 a[18:19], v9 offset:36864\n"
    "ds_read_b64_tr_b16 a[82:83], v11 offset:36864\n"
    "ds_read_b64_tr_b16 a[20:21], v9 offset:40960\n"
    "ds_read_b64_tr_b16 a[84:85], v11 offset:40960\n"
    "ds_read_b64_tr_b16 a[22:23], v9 offset:45056\n"
    "ds_read_b64_tr_b16 a[86:87], v11 offset:45056\n"
    "ds_read_b64_tr_b16 a[24:25], v9 offset:49152\n"
    "ds_read_b64_tr_b16 a[88:89], v11 offset:49152\n"
    "ds_read_b64_tr_b16 a[26:27], v9 offset:53248\n"
    "ds_read_b64_tr_b16 a[90:91], v11 offset:53248\n"
    "ds_read_b64_tr_b16 a[28:29], v9 offset:57344\n"
    "ds_read_b64_tr_b16 a[92:93], v11 offset:57344\n"
    "ds_read_b64_tr_b16 a[30:31], v9 offset:61440\n"
    "ds_read_b64_tr_b16 a[94:95], v11 offset:61440\n"
    "s_waitcnt lgkmcnt(0)\n"
    "s_barrier\n"
    "s_cmp_lg_u32 s60, 0\n"
    "s_cbranch_scc1 LSH_CI_%=\n"
    "v_mov_b32 v64, s98\n"
    "v_mov_b32 v65, s99\n"
    "v_mov_b32 v66, s98\n"
    "v_mov_b32 v67, s99\n"
    "s_lshl_b32 s52, %10, 7\n"
    "s_add_u32 s52, s52, 98304\n"
    "s_mov_b64 exec, 0xff\n"
    "buffer_store_dwordx4 v[64:67], v0, s[40:43], s52 offen sc1\n"
    "s_mov_b64 exec, -1\n"
    "LSH_CI_%=:\n"
    "s_mov_b64 s[72:73], %8\n"
    "s_add_u32 m0, s62, 0\n"
    "s_add_u32 s52, s47, 0x0\n"
    "buffer_load_dwordx4 v8, s[72:75], s52 offen lds\n"
    "s_add_u32 m0, m0, 4096\n"
    "s_add_u32 s52, s47, 0x8000\n"
    "buffer_load_dwordx4 v8, s[72:75], s52 offen lds\n"
    "s_add_u32 m0, m0, 4096\n"
    "s_add_u32 s52, s47, 0x40000\n"
    "buffer_load_dwordx4 v8, s[72:75], s52 offen lds\n"
    "s_add_u32 m0, m0, 4096\n"
    "s_add_u32 s52, s47, 0x48000\n"
    "buffer_load_dwordx4 v8, s[72:75], s52 offen lds\n"
    "s_add_u32 m0, m0, 4096\n"
    "s_add_u32 s52, s47, 0x80000\n"
    "buffer_load_dwordx4 v8, s[72:75], s52 offen lds\n"
    "s_add_u32 m0, m0, 4096\n"
    "s_add_u32 s52, s47, 0x88000\n"
    "buffer_load_dwordx4 v8, s[72:75], s52 offen lds\n"
    "s_add_u32 m0, m0, 4096\n"
    "s_add_u32 s52, s47, 0xc0000\n"
    "buffer_load_dwordx4 v8, s[72:75], s52 offen lds\n"
    "s_add_u32 m0, m0, 4096\n"
    "s_add_u32 s52, s47, 0xc8000\n"
    "buffer_load_dwordx4 v8, s[72:75], s52 offen lds\n"
    "s_add_u32 m0, m0, 4096\n"
    "s_add_u32 s52, s47, 0x100000\n"
    "buffer_load_dwordx4 v8, s[72:75], s52 offen lds\n"
    "s_add_u32 m0, m0, 4096\n"
    "s_add_u32 s52, s47, 0x108000\n"
    "buffer_load_dwordx4 v8, s[72:75], s52 offen lds\n"
    "s_add_u32 m0, m0, 4096\n"
    "s_add_u32 s52, s47, 0x140000\n"
    "buffer_load_dwordx4 v8, s[72:75], s52 offen lds\n"
    "s_add_u32 m0, m0, 4096\n"
    "s_add_u32 s52, s47, 0x148000\n"
    "buffer_load_dwordx4 v8, s[72:75], s52 offen lds\n"
    "s_add_u32 m0, m0, 4096\n"
    "s_add_u32 s52, s47, 0x180000\n"
    "buffer_load_dwordx4 v8, s[72:75], s52 offen lds\n"
    "s_add_u32 m0, m0, 4096\n"
    "s_add_u32 s52, s47, 0x188000\n"
    "buffer_load_dwordx4 v8, s[72:75], s52 offen lds\n"
    "s_add_u32 m0, m0, 4096\n"
    "s_add_u32 s52, s47, 0x1c0000\n"
    "buffer_load_dwordx4 v8, s[72:75], s52 offen lds\n"
    "s_add_u32 m0, m0, 4096\n"
    "s_add_u32 s52, s47, 0x1c8000\n"
    "buffer_load_dwordx4 v8, s[72:75], s52 offen lds\n"
    "s_waitcnt vmcnt(16)\n"
    "s_barrier\n"
    "v_add_u32 v9, 0x10000, v9\n"
    "v_add_u32 v11, 0x10000, v11\n"
    "ds_read_b64_tr_b16 a[32:33], v9 offset:0\n"
    "ds_read_b64_tr_b16 a[96:97], v11 offset:0\n"
    "ds_read_b64_tr_b16 a[34:35], v9 offset:4096\n"
    "ds_read_b64_tr_b16 a[98:99], v11 offset:4096\n"
    "ds_read_b64_tr_b16 a[36:37], v9 offset:8192\n"
    "ds_read_b64_tr_b16 a[100:101], v11 offset:8192\n"
    "ds_read_b64_tr_b16 a[38:39], v9 offset:12288\n"
    "ds_read_b64_tr_b16 a[102:103], v11 offset:12288\n"
    "ds_read_b64_tr_b16 a[40:41], v9 offset:16384\n"
    "ds_read_b64_tr_b16 a[104:105], v11 offset:16384\n"
    "ds_read_b64_tr_b16 a[42:43], v9 offset:20480\n"
    "ds_read_b64_tr_b16 a[106:107], v11 offset:20480\n"
    "ds_read_b64_tr_b16 a[44:45], v9 offset:24576\n"
    "ds_read_b64_tr_b16 a[108:109], v11 offset:24576\n"
    "ds_read_b64_tr_b16 a[46:47], v9 offset:28672\n"
    "ds_read_b64_tr_b16 a[110:111], v11 offset:28672\n"
    "ds_read_b64_tr_b16 a[48:49], v9 offset:32768\n"
    "ds_read_b64_tr_b16 a[112:113], v11 offset:32768\n"
    "ds_read_b64_tr_b16 a[50:51], v9 offset:36864\n"
    "ds_read_b64_tr_b16 a[114:115], v11 offset:36864\n"
    "ds_read_b64_tr_b16 a[52:53], v9 offset:40960\n"
    "ds_read_b64_tr_b16 a[116:117], v11 offset:40960\n"
    "ds_read_b64_tr_b16 a[54:55], v9 offset:45056\n"
    "ds_read_b64_tr_b16 a[118:119], v11 offset:45056\n"
    "ds_read_b64_tr_b16 a[56:57], v9 offset:49152\n"
    "ds_read_b64_tr_b16 a[120:121], v11 offset:49152\n"
    "ds_read_b64_tr_b16 a[58:59], v9 offset:53248\n"
    "ds_read_b64_tr_b16 a[122:123], v11 offset:53248\n"
    "ds_read_b64_tr_b16 a[60:61], v9 offset:57344\n"
    "ds_read_b64_tr_b16 a[124:125], v11 offset:57344\n"
    "ds_read_b64_tr_b16 a[62:63], v9 offset:61440\n"
    "ds_read_b64_tr_b16 a[126:127], v11 offset:61440\n"
    "v_subrev_u32 v9, 0x10000, v9\n"
    "v_subrev_u32 v11, 0x10000, v11\n"
    "s_waitcnt lgkmcnt(0)\n"
    "s_barrier\n"
    "s_mov_b64 s[72:73], %8\n"
    "s_add_u32 m0, s62, 65536\n"
    "s_add_u32 s52, s47, 0x200000\n"
    "buffer_load_dwordx4 v8, s[72:75], s52 offen lds\n"
    "s_add_u32 m0, m0, 4096\n"
    "s_add_u32 s52, s47, 0x208000\n"
    "buffer_load_dwordx4 v8, s[72:75], s52 offen lds\n"
    "s_add_u32 m0, m0, 4096\n"
    "s_add_u32 s52, s47, 0x240000\n"
    "buffer_load_dwordx4 v8, s[72:75], s52 offen lds\n"
    "s_add_u32 m0, m0, 4096\n"
    "s_add_u32 s52, s47, 0x248000\n"
    "buffer_load_dwordx4 v8, s[72:75], s52 offen lds\n"
    "s_add_u32 m0, m0, 4096\n"
    "s_add_u32 s52, s47, 0x280000\n"
    "buffer_load_dwordx4 v8, s[72:75], s52 offen lds\n"
    "s_add_u32 m0, m0, 4096\n"
    "s_add_u32 s52, s47, 0x288000\n"
    "buffer_load_dwordx4 v8, s[72:75], s52 offen lds\n"
    "s_add_u32 m0, m0, 4096\n"
    "s_add_u32 s52, s47, 0x2c0000\n"
    "buffer_load_dwordx4 v8, s[72:75], s52 offen lds\n"
    "s_add_u32 m0, m0, 4096\n"
    "s_add_u32 s52, s47, 0x2c8000\n"
    "buffer_load_dwordx4 v8, s[72:75], s52 offen lds\n"
    "s_add_u32 m0, m0, 4096\n"
    "s_add_u32 s52, s47, 0x300000\n"
    "buffer_load_dwordx4 v8, s[72:75], s52 offen lds\n"
    "s_add_u32 m0, m0, 4096\n"
    "s_add_u32 s52, s47, 0x308000\n"
    "buffer_load_dwordx4 v8, s[72:75], s52 offen lds\n"
    "s_add_u32 m0, m0, 4096\n"
    "s_add_u32 s52, s47, 0x340000\n"
    "buffer_load_dwordx4 v8, s[72:75], s52 offen lds\n"
    "s_add_u32 m0, m0, 4096\n"
    "s_add_u32 s52, s47, 0x348000\n"
    "buffer_load_dwordx4 v8, s[72:75], s52 offen lds\n"
    "s_add_u32 m0, m0, 4096\n"
    "s_add_u32 s52, s47, 0x380000\n"
    "buffer_load_dwordx4 v8, s[72:75], s52 offen lds\n"
    "s_add_u32 m0, m0, 4096\n"
    "s_add_u32 s52, s47, 0x388000\n"
    "buffer_load_dwordx4 v8, s[72:75], s52 offen lds\n"
    "s_add_u32 m0, m0, 4096\n"
    "s_add_u32 s52, s47, 0x3c0000\n"
    "buffer_load_dwordx4 v8, s[72:75], s52 offen lds\n"
    "s_add_u32 m0, m0, 4096\n"
    "s_add_u32 s52, s47, 0x3c8000\n"
    "buffer_load_dwordx4 v8, s[72:75], s52 offen lds\n"
    "s_waitcnt vmcnt(16)\n"
    "s_barrier\n"
    "ds_read_b64_tr_b16 v[128:129], v9 offset:0\n"
    "ds_read_b64_tr_b16 v[192:193], v11 offset:0\n"
    "ds_read_b64_tr_b16 v[130:131], v9 offset:4096\n"
    "ds_read_b64_tr_b16 v[194:195], v11 offset:4096\n"
    "ds_read_b64_tr_b16 v[132:133], v9 offset:8192\n"
    "ds_read_b64_tr_b16 v[196:197], v11 offset:8192\n"
    "ds_read_b64_tr_b16 v[134:135], v9 offset:12288\n"
    "ds_read_b64_tr_b16 v[198:199], v11 offset:12288\n"
    "ds_read_b64_tr_b16 v[136:137], v9 offset:16384\n"
    "ds_read_b64_tr_b16 v[200:201], v11 offset:16384\n"
    "ds_read_b64_tr_b16 v[138:139], v9 offset:20480\n"
    "ds_read_b64_tr_b16 v[202:203], v11 offset:20480\n"
    "ds_read_b64_tr_b16 v[140:141], v9 offset:24576\n"
    "ds_read_b64_tr_b16 v[204:205], v11 offset:24576\n"
    "ds_read_b64_tr_b16 v[142:143], v9 offset:28672\n"
    "ds_read_b64_tr_b16 v[206:207], v11 offset:28672\n"
    "ds_read_b64_tr_b16 v[144:145], v9 offset:32768\n"
    "ds_read_b64_tr_b16 v[208:209], v11 offset:32768\n"
    "ds_read_b64_tr_b16 v[146:147], v9 offset:36864\n"
    "ds_read_b64_tr_b16 v[210:211], v11 offset:36864\n"
    "ds_read_b64_tr_b16 v[148:149], v9 offset:40960\n"
    "ds_read_b64_tr_b16 v[212:213], v11 offset:40960\n"
    "ds_read_b64_tr_b16 v[150:151], v9 offset:45056\n"
    "ds_read_b64_tr_b16 v[214:215], v11 offset:45056\n"
    "ds_read_b64_tr_b16 v[152:153], v9 offset:49152\n"
    "ds_read_b64_tr_b16 v[216:217], v11 offset:49152\n"
    "ds_read_b64_tr_b16 v[154:155], v9 offset:53248\n"
    "ds_read_b64_tr_b16 v[218:219], v11 offset:53248\n"
    "ds_read_b64_tr_b16 v[156:157], v9 offset:57344\n"
    "ds_read_b64_tr_b16 v[220:221], v11 offset:57344\n"
    "ds_read_b64_tr_b16 v[158:159], v9 offset:61440\n"
    "ds_read_b64_tr_b16 v[222:223], v11 offset:61440\n"
    "s_waitcnt lgkmcnt(0)\n"
    "s_barrier\n"
    "s_mov_b64 s[72:73], %6\n"
    "s_add_u32 m0, s62, 0\n"
    "s_add_u32 s52, s47, 0x0\n"
    "buffer_load_dwordx4 v8, s[72:75], s52 offen lds\n"
    "s_add_u32 m0, m0, 4096\n"
    "s_add_u32 s52, s47, 0x8000\n"
    "buffer_load_dwordx4 v8, s[72:75], s52 offen lds\n"
    "s_add_u32 m0, m0, 4096\n"
    "s_add_u32 s52, s47, 0x40000\n"
    "buffer_load_dwordx4 v8, s[72:75], s52 offen lds\n"
    "s_add_u32 m0, m0, 4096\n"
    "s_add_u32 s52, s47, 0x48000\n"
    "buffer_load_dwordx4 v8, s[72:75], s52 offen lds\n"
    "s_add_u32 m0, m0, 4096\n"
    "s_add_u32 s52, s47, 0x80000\n"
    "buffer_load_dwordx4 v8, s[72:75], s52 offen lds\n"
    "s_add_u32 m0, m0, 4096\n"
    "s_add_u32 s52, s47, 0x88000\n"
    "buffer_load_dwordx4 v8, s[72:75], s52 offen lds\n"
    "s_add_u32 m0, m0, 4096\n"
    "s_add_u32 s52, s47, 0xc0000\n"
    "buffer_load_dwordx4 v8, s[72:75], s52 offen lds\n"
    "s_add_u32 m0, m0, 4096\n"
    "s_add_u32 s52, s47, 0xc8000\n"
    "buffer_load_dwordx4 v8, s[72:75], s52 offen lds\n"
    "s_add_u32 m0, m0, 4096\n"
    "s_add_u32 s52, s47, 0x100000\n"
    "buffer_load_dwordx4 v8, s[72:75], s52 offen lds\n"
    "s_add_u32 m0, m0, 4096\n"
    "s_add_u32 s52, s47, 0x108000\n"
    "buffer_load_dwordx4 v8, s[72:75], s52 offen lds\n"
    "s_add_u32 m0, m0, 4096\n"
    "s_add_u32 s52, s47, 0x140000\n"
    "buffer_load_dwordx4 v8, s[72:75], s52 offen lds\n"
    "s_add_u32 m0, m0, 4096\n"
    "s_add_u32 s52, s47, 0x148000\n"
    "buffer_load_dwordx4 v8, s[72:75], s52 offen lds\n"
    "s_add_u32 m0, m0, 4096\n"
    "s_add_u32 s52, s47, 0x180000\n"
    "buffer_load_dwordx4 v8, s[72:75], s52 offen lds\n"
    "s_add_u32 m0, m0, 4096\n"
    "s_add_u32 s52, s47, 0x188000\n"
    "buffer_load_dwordx4 v8, s[72:75], s52 offen lds\n"
    "s_add_u32 m0, m0, 4096\n"
    "s_add_u32 s52, s47, 0x1c0000\n"
    "buffer_load_dwordx4 v8, s[72:75], s52 offen lds\n"
    "s_add_u32 m0, m0, 4096\n"
    "s_add_u32 s52, s47, 0x1c8000\n"
    "buffer_load_dwordx4 v8, s[72:75], s52 offen lds\n"
    "s_waitcnt vmcnt(16)\n"
    "s_barrier\n"
    "v_add_u32 v9, 0x10000, v9\n"
    "v_add_u32 v11, 0x10000, v11\n"
    "ds_read_b64_tr_b16 v[160:161], v9 offset:0\n"
    "ds_read_b64_tr_b16 v[224:225], v11 offset:0\n"
    "ds_read_b64_tr_b16 v[162:163], v9 offset:4096\n"
    "ds_read_b64_tr_b16 v[226:227], v11 offset:4096\n"
    "ds_read_b64_tr_b16 v[164:165], v9 offset:8192\n"
    "ds_read_b64_tr_b16 v[228:229], v11 offset:8192\n"
    "ds_read_b64_tr_b16 v[166:167], v9 offset:12288\n"
    "ds_read_b64_tr_b16 v[230:231], v11 offset:12288\n"
    "ds_read_b64_tr_b16 v[168:169], v9 offset:16384\n"
    "ds_read_b64_tr_b16 v[232:233], v11 offset:16384\n"
    "ds_read_b64_tr_b16 v[170:171], v9 offset:20480\n"
    "ds_read_b64_tr_b16 v[234:235], v11 offset:20480\n"
    "ds_read_b64_tr_b16 v[172:173], v9 offset:24576\n"
    "ds_read_b64_tr_b16 v[236:237], v11 offset:24576\n"
    "ds_read_b64_tr_b16 v[174:175], v9 offset:28672\n"
    "ds_read_b64_tr_b16 v[238:239], v11 offset:28672\n"
    "ds_read_b64_tr_b16 v[176:177], v9 offset:32768\n"
    "ds_read_b64_tr_b16 v[240:241], v11 offset:32768\n"
    "ds_read_b64_tr_b16 v[178:179], v9 offset:36864\n"
    "ds_read_b64_tr_b16 v[242:243], v11 offset:36864\n"
    "ds_read_b64_tr_b16 v[180:181], v9 offset:40960\n"
    "ds_read_b64_tr_b16 v[244:245], v11 offset:40960\n"
    "ds_read_b64_tr_b16 v[182:183], v9 offset:45056\n"
    "ds_read_b64_tr_b16 v[246:247], v11 offset:45056\n"
    "ds_read_b64_tr_b16 v[184:185], v9 offset:49152\n"
    "ds_read_b64_tr_b16 v[248:249], v11 offset:49152\n"
    "ds_read_b64_tr_b16 v[186:187], v9 offset:53248\n"
    "ds_read_b64_tr_b16 v[250:251], v11 offset:53248\n"
    "ds_read_b64_tr_b16 v[188:189], v9 offset:57344\n"
    "ds_read_b64_tr_b16 v[252:253], v11 offset:57344\n"
    "ds_read_b64_tr_b16 v[190:191], v9 offset:61440\n"
    "ds_read_b64_tr_b16 v[254:255], v11 offset:61440\n"
    "v_subrev_u32 v9, 0x10000, v9\n"
    "v_subrev_u32 v11, 0x10000, v11\n"
    "s_waitcnt lgkmcnt(0)\n"
    "s_barrier\n"
    "s_mov_b64 s[72:73], %6\n"
    "s_add_u32 m0, s62, 65536\n"
    "s_add_u32 s52, s47, 0x200000\n"
    "buffer_load_dwordx4 v8, s[72:75], s52 offen lds\n"
    "s_add_u32 m0, m0, 4096\n"
    "s_add_u32 s52, s47, 0x208000\n"
    "buffer_load_dwordx4 v8, s[72:75], s52 offen lds\n"
    "s_add_u32 m0, m0, 4096\n"
    "s_add_u32 s52, s47, 0x240000\n"
    "buffer_load_dwordx4 v8, s[72:75], s52 offen lds\n"
    "s_add_u32 m0, m0, 4096\n"
    "s_add_u32 s52, s47, 0x248000\n"
    "buffer_load_dwordx4 v8, s[72:75], s52 offen lds\n"
    "s_add_u32 m0, m0, 4096\n"
    "s_add_u32 s52, s47, 0x280000\n"
    "buffer_load_dwordx4 v8, s[72:75], s52 offen lds\n"
    "s_add_u32 m0, m0, 4096\n"
    "s_add_u32 s52, s47, 0x288000\n"
    "buffer_load_dwordx4 v8, s[72:75], s52 offen lds\n"
    "s_add_u32 m0, m0, 4096\n"
    "s_add_u32 s52, s47, 0x2c0000\n"
    "buffer_load_dwordx4 v8, s[72:75], s52 offen lds\n"
    "s_add_u32 m0, m0, 4096\n"
    "s_add_u32 s52, s47, 0x2c8000\n"
    "buffer_load_dwordx4 v8, s[72:75], s52 offen lds\n"
    "s_add_u32 m0, m0, 4096\n"
    "s_add_u32 s52, s47, 0x300000\n"
    "buffer_load_dwordx4 v8, s[72:75], s52 offen lds\n"
    "s_add_u32 m0, m0, 4096\n"
    "s_add_u32 s52, s47, 0x308000\n"
    "buffer_load_dwordx4 v8, s[72:75], s52 offen lds\n"
    "s_add_u32 m0, m0, 4096\n"
    "s_add_u32 s52, s47, 0x340000\n"
    "buffer_load_dwordx4 v8, s[72:75], s52 offen lds\n"
    "s_add_u32 m0, m0, 4096\n"
    "s_add_u32 s52, s47, 0x348000\n"
    "buffer_load_dwordx4 v8, s[72:75], s52 offen lds\n"
    "s_add_u32 m0, m0, 4096\n"
    "s_add_u32 s52, s47, 0x380000\n"
    "buffer_load_dwordx4 v8, s[72:75], s52 offen lds\n"
    "s_add_u32 m0, m0, 4096\n"
    "s_add_u32 s52, s47, 0x388000\n"
    "buffer_load_dwordx4 v8, s[72:75], s52 offen lds\n"
    "s_add_u32 m0, m0, 4096\n"
    "s_add_u32 s52, s47, 0x3c0000\n"
    "buffer_load_dwordx4 v8, s[72:75], s52 offen lds\n"
    "s_add_u32 m0, m0, 4096\n"
    "s_add_u32 s52, s47, 0x3c8000\n"
    "buffer_load_dwordx4 v8, s[72:75], s52 offen lds\n"
    "s_waitcnt vmcnt(16)\n"
    "s_barrier\n"
    "ds_read_b64_tr_b16 a[128:129], v9 offset:0\n"
    "ds_read_b64_tr_b16 a[192:193], v11 offset:0\n"
    "ds_read_b64_tr_b16 a[130:131], v9 offset:4096\n"
    "ds_read_b64_tr_b16 a[194:195], v11 offset:4096\n"
    "ds_read_b64_tr_b16 a[132:133], v9 offset:8192\n"
    "ds_read_b64_tr_b16 a[196:197], v11 offset:8192\n"
    "ds_read_b64_tr_b16 a[134:135], v9 offset:12288\n"
    "ds_read_b64_tr_b16 a[198:199], v11 offset:12288\n"
    "ds_read_b64_tr_b16 a[136:137], v9 offset:16384\n"
    "ds_read_b64_tr_b16 a[200:201], v11 offset:16384\n"
    "ds_read_b64_tr_b16 a[138:139], v9 offset:20480\n"
    "ds_read_b64_tr_b16 a[202:203], v11 offset:20480\n"
    "ds_read_b64_tr_b16 a[140:141], v9 offset:24576\n"
    "ds_read_b64_tr_b16 a[204:205], v11 offset:24576\n"
    "ds_read_b64_tr_b16 a[142:143], v9 offset:28672\n"
    "ds_read_b64_tr_b16 a[206:207], v11 offset:28672\n"
    "ds_read_b64_tr_b16 a[144:145], v9 offset:32768\n"
    "ds_read_b64_tr_b16 a[208:209], v11 offset:32768\n"
    "ds_read_b64_tr_b16 a[146:147], v9 offset:36864\n"
    "ds_read_b64_tr_b16 a[210:211], v11 offset:36864\n"
    "ds_read_b64_tr_b16 a[148:149], v9 offset:40960\n"
    "ds_read_b64_tr_b16 a[212:213], v11 offset:40960\n"
    "ds_read_b64_tr_b16 a[150:151], v9 offset:45056\n"
    "ds_read_b64_tr_b16 a[214:215], v11 offset:45056\n"
    "ds_read_b64_tr_b16 a[152:153], v9 offset:49152\n"
    "ds_read_b64_tr_b16 a[216:217], v11 offset:49152\n"
    "ds_read_b64_tr_b16 a[154:155], v9 offset:53248\n"
    "ds_read_b64_tr_b16 a[218:219], v11 offset:53248\n"
    "ds_read_b64_tr_b16 a[156:157], v9 offset:57344\n"
    "ds_read_b64_tr_b16 a[220:221], v11 offset:57344\n"
    "ds_read_b64_tr_b16 a[158:159], v9 offset:61440\n"
    "ds_read_b64_tr_b16 a[222:223], v11 offset:61440\n"
    "s_waitcnt lgkmcnt(0)\n"
    "s_barrier\n"
    "v_lshrrev_b32 v18, 5, v0\n"
    "v_min_u32 v18, 2, v18\n"
    "v_lshlrev_b32 v18, 9, v18\n"
    "v_and_b32 v19, 16, v0\n"
    "v_lshl_or_b32 v18, v19, 3, v18\n"
    "s_mov_b32 s58, 0\n"
    "s_mov_b32 s52, 8192\n"
    "buffer_load_dwordx2 v[20:21], off, s[40:43], s52 sc1\n"
    "buffer_load_dwordx2 v[16:17], v18, s[40:43], s53 offen sc1\n"
    "s_add_u32 s58, s58, 1\n"
    "s_cmp_gt_u32 s58, 0x40000\n"
    "s_cbranch_scc1 LSH_FAIL_%=\n"
    "s_waitcnt vmcnt(0)\n"
    "v_xor_b32 v16, s98, v16\n"
    "v_xor_b32 v17, s99, v17\n"
    "v_xor_b32 v20, s98, v20\n"
    "v_xor_b32 v21, s99, v21\n"
    "v_and_b32 v19, 0x7fffffff, v21\n"
    "v_or3_b32 v16, v16, v17, v20\n"
    "v_or_b32 v16, v16, v19\n"
    "v_cmp_ne_u32 vcc, 0, v16\n"
    "v_readfirstlane_b32 s57, v21\n"
    "s_nop 1\n"
    "s_mov_b64 s[66:67], vcc\n"
    "s_cbranch_vccnz LSH_S1_%=\n"
    "s_lshl_b32 s54, 0, 16\n"
    "s_lshl_b32 s52, s59, 15\n"
    "s_add_u32 s54, s54, s52\n"
    "s_add_u32 s54, s54, s65\n"
    "s_add_u32 s54, s54, 0x400000\n"
    "s_add_u32 s55, s54, 4096\n"
    "s_add_u32 m0, s65, 0\n"
    "s_nop 0\n"
    "buffer_load_dwordx4 v0, s[40:43], s54 offen offset:0 sc1 lds\n"
    "buffer_load_dwordx4 v0, s[40:43], s54 offen offset:1024 sc1 lds\n"
    "buffer_load_dwordx4 v0, s[40:43], s54 offen offset:2048 sc1 lds\n"
    "buffer_load_dwordx4 v0, s[40:43], s54 offen offset:3072 sc1 lds\n"
    "s_add_u32 m0, s65, 4096\n"
    "s_nop 0\n"
    "buffer_load_dwordx4 v0, s[40:43], s55 offen offset:0 sc1 lds\n"
    "buffer_load_dwordx4 v0, s[40:43], s55 offen offset:1024 sc1 lds\n"
    "buffer_load_dwordx4 v0, s[40:43], s55 offen offset:2048 sc1 lds\n"
    "buffer_load_dwordx4 v0, s[40:43], s55 offen offset:3072 sc1 lds\n"
    "s_lshl_b32 s54, 1, 16\n"
    "s_lshl_b32 s52, s59, 15\n"
    "s_add_u32 s54, s54, s52\n"
    "s_add_u32 s54, s54, s65\n"
    "s_add_u32 s54, s54, 0x400000\n"
    "s_add_u32 s55, s54, 4096\n"
    "s_add_u32 m0, s65, 32768\n"
    "s_nop 0\n"
    "buffer_load_dwordx4 v0, s[40:43], s54 offen offset:0 sc1 lds\n"
    "buffer_load_dwordx4 v0, s[40:43], s54 offen offset:1024 sc1 lds\n"
    "buffer_load_dwordx4 v0, s[40:43], s54 offen offset:2048 sc1 lds\n"
    "buffer_load_dwordx4 v0, s[40:43], s54 offen offset:3072 sc1 lds\n"
    "s_add_u32 m0, s65, 36864\n"
    "s_nop 0\n"
    "buffer_load_dwordx4 v0, s[40:43], s55 offen offset:0 sc1 lds\n"
    "buffer_load_dwordx4 v0, s[40:43], s55 offen offset:1024 sc1 lds\n"
    "buffer_load_dwordx4 v0, s[40:43], s55 offen offset:2048 sc1 lds\n"
    "buffer_load_dwordx4 v0, s[40:43], s55 offen offset:3072 sc1 lds\n"
    "LSH_S1_%=:\n"
    "s_barrier\n"
    "v_add_u32 v9, 0x10000, v9\n"
    "v_add_u32 v11, 0x10000, v11\n"
    "ds_read_b64_tr_b16 a[160:161], v9 offset:0\n"
    "ds_read_b64_tr_b16 a[224:225], v11 offset:0\n"
    "ds_read_b64_tr_b16 a[162:163], v9 offset:4096\n"
    "ds_read_b64_tr_b16 a[226:227], v11 offset:4096\n"
    "ds_read_b64_tr_b16 a[164:165], v9 offset:8192\n"
    "ds_read_b64_tr_b16 a[228:229], v11 offset:8192\n"
    "ds_read_b64_tr_b16 a[166:167], v9 offset:12288\n"
    "ds_read_b64_tr_b16 a[230:231], v11 offset:12288\n"
    "ds_read_b64_tr_b16 a[168:169], v9 offset:16384\n"
    "ds_read_b64_tr_b16 a[232:233], v11 offset:16384\n"
    "ds_read_b64_tr_b16 a[170:171], v9 offset:20480\n"
    "ds_read_b64_tr_b16 a[234:235], v11 offset:20480\n"
    "ds_read_b64_tr_b16 a[172:173], v9 offset:24576\n"
    "ds_read_b64_tr_b16 a[236:237], v11 offset:24576\n"
    "ds_read_b64_tr_b16 a[174:175], v9 offset:28672\n"
    "ds_read_b64_tr_b16 a[238:239], v11 offset:28672\n"
    "ds_read_b64_tr_b16 a[176:177], v9 offset:32768\n"
    "ds_read_b64_tr_b16 a[240:241], v11 offset:32768\n"
    "ds_read_b64_tr_b16 a[178:179], v9 offset:36864\n"
    "ds_read_b64_tr_b16 a[242:243], v11 offset:36864\n"
    "ds_read_b64_tr_b16 a[180:181], v9 offset:40960\n"
    "ds_read_b64_tr_b16 a[244:245], v11 offset:40960\n"
    "ds_read_b64_tr_b16 a[182:183], v9 offset:45056\n"
    "ds_read_b64_tr_b16 a[246:247], v11 offset:45056\n"
    "ds_read_b64_tr_b16 a[184:185], v9 offset:49152\n"
    "ds_read_b64_tr_b16 a[248:249], v11 offset:49152\n"
    "ds_read_b64_tr_b16 a[186:187], v9 offset:53248\n"
    "ds_read_b64_tr_b16 a[250:251], v11 offset:53248\n"
    "ds_read_b64_tr_b16 a[188:189], v9 offset:57344\n"
    "ds_read_b64_tr_b16 a[252:253], v11 offset:57344\n"
    "ds_read_b64_tr_b16 a[190:191], v9 offset:61440\n"
    "ds_read_b64_tr_b16 a[254:255], v11 offset:61440\n"
    "v_subrev_u32 v9, 0x10000, v9\n"
    "v_subrev_u32 v11, 0x10000, v11\n"
    "s_waitcnt lgkmcnt(0)\n"
    "s_barrier\n"
    "s_cmp_eq_u64 s[66:67], 0\n"
    "s_cbranch_scc1 LSH_S2_%=\n"
    "LSH_CAL_%=:\n"
    "s_mov_b32 s52, 8192\n"
    "buffer_load_dwordx2 v[20:21], off, s[40:43], s52 sc1\n"
    "buffer_load_dwordx2 v[16:17], v18, s[40:43], s53 offen sc1\n"
    "s_add_u32 s58, s58, 1\n"
    "s_cmp_gt_u32 s58, 0x40000\n"
    "s_cbranch_scc1 LSH_FAIL_%=\n"
    "s_waitcnt vmcnt(0)\n"
    "v_xor_b32 v16, s98, v16\n"
    "v_xor_b32 v17, s99, v17\n"
    "v_xor_b32 v20, s98, v20\n"
    "v_xor_b32 v21, s99, v21\n"
    "v_and_b32 v19, 0x7fffffff, v21\n"
    "v_or3_b32 v16, v16, v17, v20\n"
    "v_or_b32 v16, v16, v19\n"
    "v_cmp_ne_u32 vcc, 0, v16\n"
    "v_readfirstlane_b32 s57, v21\n"
    "s_nop 1\n"
    "s_cbranch_vccnz LSH_CAL_%=\n"
    "s_lshl_b32 s54, 0, 16\n"
    "s_lshl_b32 s52, s59, 15\n"
    "s_add_u32 s54, s54, s52\n"
    "s_add_u32 s54, s54, s65\n"
    "s_add_u32 s54, s54, 0x400000\n"
    "s_add_u32 s55, s54, 4096\n"
    "s_add_u32 m0, s65, 0\n"
    "s_nop 0\n"
    "buffer_load_dwordx4 v0, s[40:43], s54 offen offset:0 sc1 lds\n"
    "buffer_load_dwordx4 v0, s[40:43], s54 offen offset:1024 sc1 lds\n"
    "buffer_load_dwordx4 v0, s[40:43], s54 offen offset:2048 sc1 lds\n"
    "buffer_load_dwordx4 v0, s[40:43], s54 offen offset:3072 sc1 lds\n"
    "s_add_u32 m0, s65, 4096\n"
    "s_nop 0\n"
    "buffer_load_dwordx4 v0, s[40:43], s55 offen offset:0 sc1 lds\n"
    "buffer_load_dwordx4 v0, s[40:43], s55 offen offset:1024 sc1 lds\n"
    "buffer_load_dwordx4 v0, s[40:43], s55 offen offset:2048 sc1 lds\n"
    "buffer_load_dwordx4 v0, s[40:43], s55 offen offset:3072 sc1 lds\n"
    "s_lshl_b32 s54, 1, 16\n"
    "s_lshl_b32 s52, s59, 15\n"
    "s_add_u32 s54, s54, s52\n"
    "s_add_u32 s54, s54, s65\n"
    "s_add_u32 s54, s54, 0x400000\n"
    "s_add_u32 s55, s54, 4096\n"
    "s_add_u32 m0, s65, 32768\n"
    "s_nop 0\n"
    "buffer_load_dwordx4 v0, s[40:43], s54 offen offset:0 sc1 lds\n"
    "buffer_load_dwordx4 v0, s[40:43], s54 offen offset:1024 sc1 lds\n"
    "buffer_load_dwordx4 v0, s[40:43], s54 offen offset:2048 sc1 lds\n"
    "buffer_load_dwordx4 v0, s[40:43], s54 offen offset:3072 sc1 lds\n"
    "s_add_u32 m0, s65, 36864\n"
    "s_nop 0\n"
    "buffer_load_dwordx4 v0, s[40:43], s55 offen offset:0 sc1 lds\n"
    "buffer_load_dwordx4 v0, s[40:43], s55 offen offset:1024 sc1 lds\n"
    "buffer_load_dwordx4 v0, s[40:43], s55 offen offset:2048 sc1 lds\n"
    "buffer_load_dwordx4 v0, s[40:43], s55 offen offset:3072 sc1 lds\n"
    "LSH_S2_%=:\n"
    "v_cmp_le_i32 s[50:51], 0, v10\n"
    "v_cmp_le_i32 s[78:79], 16, v10\n"
    "v_cmp_le_i32 s[72:73], 1, v10\n"
    "v_cmp_le_i32 s[90:91], 17, v10\n"
    "v_cmp_le_i32 s[74:75], 2, v10\n"
    "v_cmp_le_i32 s[92:93], 18, v10\n"
    "v_cmp_le_i32 s[76:77], 3, v10\n"
    "v_cmp_le_i32 s[94:95], 19, v10\n"
    "v_mov_b32 v120, s98\n"
    "v_mov_b32 v121, s99\n"
    "v_mov_b32 v122, 0xff800000\n"
    "v_mov_b32 v112, 0x3f803f80\n"
    "v_mov_b32 v113, 0x3f803f80\n"
    "v_mov_b32 v114, 0x3f803f80\n"
    "v_mov_b32 v115, 0x3f803f80\n"
    "s_lshr_b32 s57, s57, 31\n"
    "s_xor_b32 s57, s57, 1\n"
    "s_lshl_b32 s57, s57, 13\n"
    "s_add_u32 s70, s57, %9\n"
    "s_lshl_b32 s96, s59, 1\n"
    "s_add_u32 s96, s96, s63\n"
    "s_lshl_b32 s96, s96, 14\n"
    "s_lshl_b32 s52, s61, 1\n"
    "s_add_u32 s52, s52, s64\n"
    "s_lshl_b32 s52, s52, 7\n"
    "s_add_u32 s96, s96, s52\n"
    "s_add_u32 s96, s96, s70\n"
    "s_sub_u32 s96, s96, 135168\n"
    "s_lshl_b32 s97, s59, 1\n"
    "s_lshr_b32 s52, s61, 4\n"
    "s_add_u32 s97, s97, s52\n"
    "s_lshl_b32 s97, s97, 14\n"
    "s_and_b32 s52, s61, 15\n"
    "s_lshl_b32 s52, s52, 2\n"
    "s_add_u32 s52, s52, s60\n"
    "s_lshl_b32 s52, s52, 7\n"
    "s_add_u32 s97, s97, s52\n"
    "s_add_u32 s97, s97, s70\n"
    "s_sub_u32 s97, s97, 69632\n"
    "s_waitcnt vmcnt(8)\n"
    "s_branch LSH_P1_%=\n"
    "LSH_LOOP_%=:\n"
    "s_cmp_lt_u32 s45, 3\n"
    "s_cbranch_scc1 LSH_P1_%=\n"
    "s_cmp_lg_u32 s45, 3\n"
    "s_cbranch_scc1 LSH_P2_%=\n"
    "s_waitcnt lgkmcnt(0)\n"
    "s_barrier\n"
    "v_lshrrev_b32 v18, 5, v0\n"
    "v_min_u32 v18, 31, v18\n"
    "v_lshlrev_b32 v18, 9, v18\n"
    "v_and_b32 v19, 16, v0\n"
    "v_lshl_or_b32 v18, v19, 3, v18\n"
    "s_mov_b32 s58, 0\n"
    "LSH_H1_%=:\n"
    "buffer_load_dwordx2 v[16:17], v18, s[40:43], s53 offen sc1\n"
    "s_add_u32 s58, s58, 1\n"
    "s_cmp_gt_u32 s58, 0x40000\n"
    "s_cbranch_scc1 LSH_FAIL_%=\n"
    "s_waitcnt vmcnt(0)\n"
    "v_xor_b32 v16, s98, v16\n"
    "v_xor_b32 v17, s99, v17\n"
    "v_or_b32 v16, v16, v17\n"
    "v_cmp_ne_u32 vcc, 0, v16\n"
    "s_nop 1\n"
    "s_cbranch_vccnz LSH_H1_%=\n"
    "s_lshl_b32 s54, s45, 16\n"
    "s_lshl_b32 s52, s59, 15\n"
    "s_add_u32 s54, s54, s52\n"
    "s_add_u32 s54, s54, s65\n"
    "s_add_u32 s54, s54, 0x400000\n"
    "s_add_u32 s55, s54, 4096\n"
    "s_add_u32 m0, s65, 0\n"
    "s_nop 0\n"
    "buffer_load_dwordx4 v0, s[40:43], s54 offen offset:0 sc1 lds\n"
    "buffer_load_dwordx4 v0, s[40:43], s54 offen offset:1024 sc1 lds\n"
    "buffer_load_dwordx4 v0, s[40:43], s54 offen offset:2048 sc1 lds\n"
    "buffer_load_dwordx4 v0, s[40:43], s54 offen offset:3072 sc1 lds\n"
    "s_add_u32 m0, s65, 4096\n"
    "s_nop 0\n"
    "buffer_load_dwordx4 v0, s[40:43], s55 offen offset:0 sc1 lds\n"
    "buffer_load_dwordx4 v0, s[40:43], s55 offen offset:1024 sc1 lds\n"
    "buffer_load_dwordx4 v0, s[40:43], s55 offen offset:2048 sc1 lds\n"
    "buffer_load_dwordx4 v0, s[40:43], s55 offen offset:3072 sc1 lds\n"
    "s_waitcnt vmcnt(0)\n"
    "s_branch LSH_P1_%=\n"
    "LSH_P2_%=:\n"
    "s_lshl_b32 s56, s44, 1\n"
    "s_add_u32 s56, s56, s59\n"
    "s_add_u32 s89, s98, s56\n"
    "s_add_u32 s52, s44, 2\n"
    "s_and_b32 s52, s52, 3\n"
    "s_mul_i32 s52, s52, 24576\n"
    "s_add_u32 s52, s52, 32768\n"
    "v_add_u32 v7, s52, v0\n"
    "s_lshl_b32 s57, s63, 12\n"
    "v_add_u32 v6, s57, v7\n"
    "s_lshl_b32 s57, s64, 12\n"
    "v_add_u32 v123, s57, v7\n"
    "s_waitcnt vmcnt(1)\n"
    "s_cmp_eq_u32 s56, 0\n"
    "s_cbranch_scc1 LSH_MEMOK_%=\n"
    "v_xor_b32 v16, s89, v89\n"
    "v_xor_b32 v17, s89, v91\n"
    "v_xor_b32 v18, s89, v93\n"
    "v_or_b32 v16, v16, v18\n"
    "v_xor_b32 v18, s89, v95\n"
    "v_or_b32 v16, v16, v18\n"
    "v_xor_b32 v18, s89, v97\n"
    "v_or_b32 v16, v16, v18\n"
    "v_xor_b32 v18, s89, v99\n"
    "v_or_b32 v16, v16, v18\n"
    "v_xor_b32 v18, s89, v101\n"
    "v_or_b32 v16, v16, v18\n"
    "v_xor_b32 v18, s89, v103\n"
    "v_or_b32 v16, v16, v18\n"
    "v_or_b32 v16, v16, v17\n"
    "v_cmp_ne_u32 vcc, 0, v16\n"
    "s_nop 1\n"
    "s_cbranch_vccz LSH_SCAT_%=\n"
    "s_and_b32 s66, s56, 15\n"
    "s_lshl_b32 s66, s66, 1\n"
    "s_add_u32 s66, s66, s64\n"
    "s_lshl_b32 s66, s66, 14\n"
    "s_lshl_b32 s52, s63, 12\n"
    "s_add_u32 s66, s66, s52\n"
    "s_add_u32 s66, s66, s70\n"
    "s_add_u32 s66, s66, 0x40000\n"
    "s_lshl_b32 s52, s56, 7\n"
    "s_add_u32 s52, s52, s70\n"
    "s_add_u32 s52, s52, 196480\n"
    "s_mov_b32 s58, 0\n"
    "LSH_SW_%=:\n"
    "buffer_load_dwordx2 v[16:17], off, s[40:43], s52 sc1\n"
    "s_add_u32 s58, s58, 1\n"
    "s_waitcnt vmcnt(0)\n"
    "v_readfirstlane_b32 s67, v16\n"
    "v_readfirstlane_b32 s68, v17\n"
    "s_cmp_gt_u32 s58, 0x100\n"
    "s_cbranch_scc1 LSH_LP_%=\n"
    "s_cmp_eq_u32 s68, s98\n"
    "s_cbranch_scc1 LSH_TW_%=\n"
    "s_sleep 4\n"
    "s_branch LSH_SW_%=\n"
    "LSH_TW_%=:\n"
    "s_memrealtime s[68:69]\n"
    "s_waitcnt lgkmcnt(0)\n"
    "s_sub_u32 s52, s67, s68\n"
    "s_cmp_lt_u32 s52, 1000\n"
    "s_cbranch_scc1 LSH_TW_%=\n"
    "s_branch LSH_LD_%=\n"
    "LSH_LP_%=:\n"
    "buffer_load_dword v16, off, s[40:43], s66 offset:4092 sc1\n"
    "s_add_u32 s58, s58, 1\n"
    "s_cmp_gt_u32 s58, 0x40000\n"
    "s_cbranch_scc1 LSH_FAIL_%=\n"
    "s_waitcnt vmcnt(0)\n"
    "v_cmp_eq_u32 vcc, s89, v16\n"
    "s_nop 1\n"
    "s_cbranch_vccnz LSH_LD_%=\n"
    "s_sleep 3\n"
    "s_branch LSH_LP_%=\n"
    "LSH_LD_%=:\n"
    "buffer_load_dwordx4 v[88:91], v0, s[40:43], s66 offen offset:0 sc1\n"
    "buffer_load_dwordx4 v[92:95], v0, s[40:43], s66 offen offset:1024 sc1\n"
    "buffer_load_dwordx4 v[96:99], v0, s[40:43], s66 offen offset:2048 sc1\n"
    "buffer_load_dwordx4 v[100:103], v0, s[40:43], s66 offen offset:3072 sc1\n"
    "s_waitcnt vmcnt(0)\n"
    "v_xor_b32 v16, s89, v89\n"
    "v_xor_b32 v17, s89, v91\n"
    "v_xor_b32 v18, s89, v93\n"
    "v_or_b32 v16, v16, v18\n"
    "v_xor_b32 v18, s89, v95\n"
    "v_or_b32 v16, v16, v18\n"
    "v_xor_b32 v18, s89, v97\n"
    "v_or_b32 v16, v16, v18\n"
    "v_xor_b32 v18, s89, v99\n"
    "v_or_b32 v16, v16, v18\n"
    "v_xor_b32 v18, s89, v101\n"
    "v_or_b32 v16, v16, v18\n"
    "v_xor_b32 v18, s89, v103\n"
    "v_or_b32 v16, v16, v18\n"
    "v_or_b32 v16, v16, v17\n"
    "v_cmp_ne_u32 vcc, 0, v16\n"
    "s_nop 1\n"
    "s_cbranch_vccnz LSH_LP_%=\n"
    "LSH_SCAT_%=:\n"
    "ds_write_b16 v4, v88 offset:0\n"
    "ds_write_b16_d16_hi v4, v88 offset:256\n"
    "ds_write_b16 v4, v90 offset:512\n"
    "ds_write_b16_d16_hi v4, v90 offset:768\n"
    "ds_write_b16 v4, v92 offset:8\n"
    "ds_write_b16_d16_hi v4, v92 offset:264\n"
    "ds_write_b16 v4, v94 offset:520\n"
    "ds_write_b16_d16_hi v4, v94 offset:776\n"
    "ds_write_b16 v4, v96 offset:1024\n"
    "ds_write_b16_d16_hi v4, v96 offset:1280\n"
    "ds_write_b16 v4, v98 offset:1536\n"
    "ds_write_b16_d16_hi v4, v98 offset:1792\n"
    "ds_write_b16 v4, v100 offset:1032\n"
    "ds_write_b16_d16_hi v4, v100 offset:1288\n"
    "ds_write_b16 v4, v102 offset:1544\n"
    "ds_write_b16_d16_hi v4, v102 offset:1800\n"
    "LSH_MEMOK_%=:\n"
    "s_lshl_b32 s54, s45, 16\n"
    "s_lshl_b32 s52, s59, 15\n"
    "s_add_u32 s54, s54, s52\n"
    "s_add_u32 s54, s54, s65\n"
    "s_add_u32 s54, s54, 0x400000\n"
    "s_add_u32 s55, s54, 4096\n"
    "v_add_u32 v124, 137216, v0\n"
    "s_waitcnt lgkmcnt(0)\n"
    "s_barrier\n"
    "ds_read_b128 v[8:11], v124 offset:0\n"
    "ds_read_b128 v[12:15], v124 offset:1024\n"
    "ds_read_b128 v[16:19], v124 offset:2048\n"
    "ds_read_b128 v[20:23], v124 offset:3072\n"
    "v_mov_b64 v[88:89], 0\n"
    "v_mov_b64 v[90:91], 0\n"
    "v_mov_b64 v[92:93], 0\n"
    "v_mov_b64 v[94:95], 0\n"
    "v_mov_b64 v[96:97], 0\n"
    "v_mov_b64 v[98:99], 0\n"
    "v_mov_b64 v[100:101], 0\n"
    "v_mov_b64 v[102:103], 0\n"
    "s_waitcnt lgkmcnt(3)\n"
    "v_smfmac_f32_16x16x64_bf16 v[88:91], v[8:11], a[0:7], v1\n"
    "ds_read_b128 v[24:27], v124 offset:4096\n"
    "v_smfmac_f32_16x16x64_bf16 v[92:95], v[8:11], a[64:71], v1\n"
    "ds_read_b128 v[28:31], v124 offset:5120\n"
    "s_waitcnt lgkmcnt(4)\n"
    "v_smfmac_f32_16x16x64_bf16 v[88:91], v[12:15], a[8:15], v1\n"
    "ds_read_b128 v[32:35], v124 offset:6144\n"
    "v_smfmac_f32_16x16x64_bf16 v[92:95], v[12:15], a[72:79], v1\n"
    "ds_read_b128 v[36:39], v124 offset:7168\n"
    "s_waitcnt lgkmcnt(4)\n"
    "v_smfmac_f32_16x16x64_bf16 v[88:91], v[16:19], a[16:23], v1\n"
    "ds_read_b128 v[56:59], v6 offset:0\n"
    "v_smfmac_f32_16x16x64_bf16 v[92:95], v[16:19], a[80:87], v1\n"
    "ds_read_b128 v[60:63], v6 offset:1024\n"
    "v_smfmac_f32_16x16x64_bf16 v[88:91], v[20:23], a[24:31], v1\n"
    "ds_read_b128 v[64:67], v6 offset:2048\n"
    "v_smfmac_f32_16x16x64_bf16 v[92:95], v[20:23], a[88:95], v1\n"
    "ds_read_b128 v[68:71], v6 offset:3072\n"
    "s_waitcnt lgkmcnt(4)\n"
    "v_smfmac_f32_16x16x64_bf16 v[88:91], v[24:27], a[32:39], v1\n"
    "ds_read_b128 v[72:75], v7 offset:8192\n"
    "v_smfmac_f32_16x16x64_bf16 v[92:95], v[24:27], a[96:103], v1\n"
    "ds_read_b128 v[76:79], v7 offset:9216\n"
    "v_smfmac_f32_16x16x64_bf16 v[88:91], v[28:31], a[40:47], v1\n"
    "ds_read_b128 v[80:83], v7 offset:10240\n"
    "v_smfmac_f32_16x16x64_bf16 v[92:95], v[28:31], a[104:111], v1\n"
    "ds_read_b128 v[84:87], v7 offset:11264\n"
    "v_smfmac_f32_16x16x64_bf16 v[88:91], v[32:35], a[48:55], v1\n"
    "ds_read_b128 v[40:43], v7 offset:12288\n"
    "v_smfmac_f32_16x16x64_bf16 v[92:95], v[32:35], a[112:119], v1\n"
    "ds_read_b128 v[44:47], v7 offset:13312\n"
    "v_smfmac_f32_16x16x64_bf16 v[88:91], v[36:39], a[56:63], v1\n"
    "ds_read_b128 v[48:51], v7 offset:14336\n"
    "v_smfmac_f32_16x16x64_bf16 v[92:95], v[36:39], a[120:127], v1\n"
    "ds_read_b128 v[52:55], v7 offset:15360\n"
    "v_smfmac_f32_16x16x64_bf16 v[96:99], v[8:11], v[128:135], v1\n"
    "s_nop 0\n"
    "s_nop 0\n"
    "v_smfmac_f32_16x16x64_bf16 v[100:103], v[8:11], v[192:199], v1\n"
    "s_nop 0\n"
    "s_nop 0\n"
    "v_smfmac_f32_16x16x64_bf16 v[96:99], v[12:15], v[136:143], v1\n"
    "s_waitcnt lgkmcnt(0)\n"
    "v_add_f32 v88, v88, v90\n"
    "v_mfma_f32_16x16x32_bf16 v[104:107], v[72:75], v[56:59], 0\n"
    "v_add_f32 v89, v89, v91\n"
    "v_add_f32 v92, v92, v94\n"
    "v_mfma_f32_16x16x32_bf16 v[108:111], v[40:43], v[56:59], 0\n"
    "v_add_f32 v93, v93, v95\n"
    "v_cvt_pk_bf16_f32 v88, v88, v89\n"
    "v_mfma_f32_16x16x32_bf16 v[104:107], v[76:79], v[60:63], v[104:107]\n"
    "v_cvt_pk_bf16_f32 v92, v92, v93\n"
    "v_mfma_f32_16x16x32_bf16 v[108:111], v[44:47], v[60:63], v[108:111]\n"
    "ds_write2st64_b32 v3, v88, v92 offset1:2\n"
    "v_mfma_f32_16x16x32_bf16 v[104:107], v[80:83], v[64:67], v[104:107]\n"
    "v_mfma_f32_16x16x32_bf16 v[108:111], v[48:51], v[64:67], v[108:111]\n"
    "v_mfma_f32_16x16x32_bf16 v[104:107], v[84:87], v[68:71], v[104:107]\n"
    "v_mfma_f32_16x16x32_bf16 v[108:111], v[52:55], v[68:71], v[108:111]\n"
    "v_smfmac_f32_16x16x64_bf16 v[100:103], v[12:15], v[200:207], v1\n"
    "s_waitcnt lgkmcnt(0)\n"
    "s_barrier\n"
    "v_smfmac_f32_16x16x64_bf16 v[96:99], v[16:19], v[144:151], v1\n"
    "ds_read_b64_tr_b16 v[72:73], v2 offset:0\n"
    "v_smfmac_f32_16x16x64_bf16 v[100:103], v[16:19], v[208:215], v1\n"
    "ds_read_b64_tr_b16 v[74:75], v2 offset:512\n"
    "v_smfmac_f32_16x16x64_bf16 v[96:99], v[20:23], v[152:159], v1\n"
    "ds_read_b64_tr_b16 v[76:77], v2 offset:1024\n"
    "v_cndmask_b32 v104, v122, v104, s[50:51]\n"
    "v_smfmac_f32_16x16x64_bf16 v[100:103], v[20:23], v[216:223], v1\n"
    "ds_read_b64_tr_b16 v[78:79], v2 offset:1536\n"
    "v_cndmask_b32 v105, v122, v105, s[72:73]\n"
    "v_smfmac_f32_16x16x64_bf16 v[96:99], v[24:27], v[160:167], v1\n"
    "ds_read_b64_tr_b16 v[80:81], v2 offset:2048\n"
    "v_cndmask_b32 v106, v122, v106, s[74:75]\n"
    "v_smfmac_f32_16x16x64_bf16 v[100:103], v[24:27], v[224:231], v1\n"
    "ds_read_b64_tr_b16 v[82:83], v2 offset:2560\n"
    "v_cndmask_b32 v107, v122, v107, s[76:77]\n"
    "v_smfmac_f32_16x16x64_bf16 v[96:99], v[28:31], v[168:175], v1\n"
    "ds_read_b64_tr_b16 v[84:85], v2 offset:3072\n"
    "v_cndmask_b32 v108, v122, v108, s[78:79]\n"
    "v_smfmac_f32_16x16x64_bf16 v[100:103], v[28:31], v[232:239], v1\n"
    "ds_read_b64_tr_b16 v[86:87], v2 offset:3584\n"
    "v_cndmask_b32 v109, v122, v109, s[90:91]\n"
    "v_smfmac_f32_16x16x64_bf16 v[96:99], v[32:35], v[176:183], v1\n"
    "ds_read_b128 v[40:43], v123 offset:16384\n"
    "v_cndmask_b32 v110, v122, v110, s[92:93]\n"
    "v_smfmac_f32_16x16x64_bf16 v[100:103], v[32:35], v[240:247], v1\n"
    "ds_read_b128 v[44:47], v123 offset:17408\n"
    "v_cndmask_b32 v111, v122, v111, s[94:95]\n"
    "v_smfmac_f32_16x16x64_bf16 v[96:99], v[36:39], v[184:191], v1\n"
    "ds_read_b128 v[48:51], v123 offset:18432\n"
    "v_max3_f32 v88, v104, v105, v106\n"
    "v_smfmac_f32_16x16x64_bf16 v[100:103], v[36:39], v[248:255], v1\n"
    "ds_read_b128 v[52:55], v123 offset:19456\n"
    "s_waitcnt lgkmcnt(4)\n"
    "v_max3_f32 v89, v108, v109, v110\n"
    "v_mfma_f32_16x16x32_bf16 v[116:119], v[72:75], v[56:59], 0\n"
    "v_max3_f32 v88, v88, v107, v111\n"
    "v_mov_b64 v[8:9], 0\n"
    "v_mfma_f32_16x16x32_bf16 v[116:119], v[76:79], v[60:63], v[116:119]\n"
    "v_max_f32 v88, v88, v89\n"
    "v_mov_b64 v[10:11], 0\n"
    "v_mfma_f32_16x16x32_bf16 v[116:119], v[80:83], v[64:67], v[116:119]\n"
    "v_add_f32 v96, v96, v98\n"
    "v_add_f32 v97, v97, v99\n"
    "v_mfma_f32_16x16x32_bf16 v[116:119], v[84:87], v[68:71], v[116:119]\n"
    "v_add_f32 v100, v100, v102\n"
    "v_add_f32 v101, v101, v103\n"
    "v_cvt_pk_bf16_f32 v90, v96, v97\n"
    "v_cvt_pk_bf16_f32 v91, v100, v101\n"
    "s_lshl_b32 s57, s60, 9\n"
    "v_add_u32 v125, s57, v5\n"
    "ds_write_b64 v125, v[90:91]\n"
    "s_lshl_b32 s57, s64, 10\n"
    "v_add_u32 v125, s57, v5\n"
    "v_max3_f32 v88, v88, v116, v117\n"
    "v_mov_b32 v89, v88\n"
    "s_cmp_ge_u32 s45, 32\n"
    "s_cbranch_scc1 LSH_NOH_%=\n"
    "s_add_u32 m0, s65, 0\n"
    "s_nop 0\n"
    "buffer_load_dwordx4 v0, s[40:43], s54 offen offset:0 sc1 lds\n"
    "buffer_load_dwordx4 v0, s[40:43], s54 offen offset:1024 sc1 lds\n"
    "buffer_load_dwordx4 v0, s[40:43], s54 offen offset:2048 sc1 lds\n"
    "buffer_load_dwordx4 v0, s[40:43], s54 offen offset:3072 sc1 lds\n"
    "s_add_u32 m0, s65, 4096\n"
    "s_nop 0\n"
    "buffer_load_dwordx4 v0, s[40:43], s55 offen offset:0 sc1 lds\n"
    "buffer_load_dwordx4 v0, s[40:43], s55 offen offset:1024 sc1 lds\n"
    "buffer_load_dwordx4 v0, s[40:43], s55 offen offset:2048 sc1 lds\n"
    "buffer_load_dwordx4 v0, s[40:43], s55 offen offset:3072 sc1 lds\n"
    "LSH_NOH_%=:\n"
    "s_nop 1\n"
    "v_permlane16_swap_b32 v88, v89\n"
    "v_max_f32 v88, v88, v89\n"
    "v_mov_b32 v89, v88\n"
    "s_waitcnt lgkmcnt(0)\n"
    "s_barrier\n"
    "ds_read_b64 v[92:93], v125\n"
    "ds_read_b64 v[94:95], v125 offset:512\n"
    "v_permlane32_swap_b32 v88, v89\n"
    "v_max_f32 v88, v88, v89\n"
    "v_mul_f32 v88, 0xbe0293ee, v88\n"
    "v_fmamk_f32 v104, v104, 0x3e0293ee, v88\n"
    "v_fmamk_f32 v105, v105, 0x3e0293ee, v88\n"
    "v_fmamk_f32 v106, v106, 0x3e0293ee, v88\n"
    "v_fmamk_f32 v107, v107, 0x3e0293ee, v88\n"
    "v_fmamk_f32 v108, v108, 0x3e0293ee, v88\n"
    "v_fmamk_f32 v109, v109, 0x3e0293ee, v88\n"
    "v_fmamk_f32 v110, v110, 0x3e0293ee, v88\n"
    "v_fmamk_f32 v111, v111, 0x3e0293ee, v88\n"
    "v_fmamk_f32 v116, v116, 0x3e0293ee, v88\n"
    "v_fmamk_f32 v117, v117, 0x3e0293ee, v88\n"
    "v_exp_f32 v104, v104\n"
    "v_exp_f32 v105, v105\n"
    "v_exp_f32 v106, v106\n"
    "v_exp_f32 v107, v107\n"
    "v_exp_f32 v108, v108\n"
    "v_exp_f32 v109, v109\n"
    "v_exp_f32 v110, v110\n"
    "v_exp_f32 v111, v111\n"
    "v_exp_f32 v116, v116\n"
    "v_exp_f32 v117, v117\n"
    "v_mov_b32 v96, v112\n"
    "v_mov_b32 v97, v112\n"
    "v_cvt_pk_bf16_f32 v12, v104, v105\n"
    "v_cvt_pk_bf16_f32 v13, v106, v107\n"
    "v_cvt_pk_bf16_f32 v14, v108, v109\n"
    "v_cvt_pk_bf16_f32 v15, v110, v111\n"
    "v_cvt_pk_bf16_f32 v9, v116, v117\n"
    "v_cvt_pk_bf16_f32 v116, v116, v117\n"
    "v_mov_b32 v117, 0\n"
    "s_waitcnt lgkmcnt(0)\n"
    "v_mfma_f32_16x16x32_bf16 v[32:35], v[112:115], v[12:15], 0\n"
    "v_mfma_f32_16x16x32_bf16 v[32:35], v[112:115], v[8:11], v[32:35]\n"
    "v_mfma_f32_16x16x32_bf16 v[16:19], v[40:43], v[12:15], 0\n"
    "v_mfma_f32_16x16x32_bf16 v[20:23], v[44:47], v[12:15], 0\n"
    "v_mfma_f32_16x16x32_bf16 v[24:27], v[48:51], v[12:15], 0\n"
    "v_mfma_f32_16x16x32_bf16 v[28:31], v[52:55], v[12:15], 0\n"
    "s_lshl_b32 s52, s56, 1\n"
    "s_add_u32 s52, s52, s63\n"
    "s_add_u32 s46, s56, 1\n"
    "s_lshl_b32 s57, s52, 17\n"
    "v_mfma_f32_16x16x32_bf16 v[16:19], v[92:95], v[116:119], v[16:19]\n"
    "v_mfma_f32_16x16x32_bf16 v[20:23], v[92:95], v[8:11], v[20:23]\n"
    "v_mfma_f32_16x16x32_bf16 v[24:27], v[94:97], v[116:119], v[24:27]\n"
    "v_mfma_f32_16x16x32_bf16 v[28:31], v[94:97], v[8:11], v[28:31]\n"
    "v_rcp_f32 v88, v32\n"
    "s_lshl_b32 s52, s61, 1\n"
    "s_add_u32 s52, s52, s64\n"
    "s_nop 2\n"
    "s_lshl_b32 s52, s52, 11\n"
    "s_add_u32 s57, s57, s52\n"
    "s_add_u32 s57, s57, 0x2000000\n"
    "v_mul_f32 v16, v16, v88\n"
    "v_mul_f32 v17, v17, v88\n"
    "v_mul_f32 v18, v18, v88\n"
    "v_mul_f32 v19, v19, v88\n"
    "v_mul_f32 v20, v20, v88\n"
    "v_mul_f32 v21, v21, v88\n"
    "v_mul_f32 v22, v22, v88\n"
    "v_mul_f32 v23, v23, v88\n"
    "v_cvt_pk_bf16_f32 v104, v16, v17\n"
    "v_cvt_pk_bf16_f32 v105, v18, v19\n"
    "v_cvt_pk_bf16_f32 v106, v20, v21\n"
    "v_cvt_pk_bf16_f32 v107, v22, v23\n"
    "buffer_store_dwordx4 v[104:107], v0, s[40:43], s57 offen offset:0 sc1\n"
    "v_mul_f32 v24, v24, v88\n"
    "v_mul_f32 v25, v25, v88\n"
    "v_mul_f32 v26, v26, v88\n"
    "v_mul_f32 v27, v27, v88\n"
    "v_mul_f32 v28, v28, v88\n"
    "v_mul_f32 v29, v29, v88\n"
    "v_mul_f32 v30, v30, v88\n"
    "v_mul_f32 v31, v31, v88\n"
    "v_cvt_pk_bf16_f32 v108, v24, v25\n"
    "v_cvt_pk_bf16_f32 v109, v26, v27\n"
    "v_cvt_pk_bf16_f32 v110, v28, v29\n"
    "v_cvt_pk_bf16_f32 v111, v30, v31\n"
    "buffer_store_dwordx4 v[108:111], v0, s[40:43], s57 offen offset:1024 sc1\n"
    "v_mov_b32 v89, s89\n"
    "s_add_u32 s44, s44, 1\n"
    "s_cmp_lt_u32 s45, 32\n"
    "s_cbranch_scc1 LSH_P2E_%=\n"
    "s_waitcnt vmcnt(0)\n"
    "s_add_u32 s52, s98, s46\n"
    "v_mov_b32 v120, s52\n"
    "s_mov_b64 exec, 0xffff\n"
    "buffer_store_dwordx2 v[120:121], v5, s[40:43], s96 offen sc1\n"
    "s_mov_b64 exec, -1\n"
    "s_cmp_lt_u32 s44, 32\n"
    "s_cbranch_scc1 LSH_P2_%=\n"
    "s_branch LSH_END_%=\n"
    "LSH_P2E_%=:\n"
    "s_waitcnt vmcnt(2)\n"
    "LSH_P1_%=:\n"
    "s_cmp_eq_u32 s45, 1\n"
    "s_cselect_b32 s52, 0x8000, 0\n"
    "v_add_u32 v123, s52, v0\n"
    "s_waitcnt lgkmcnt(0)\n"
    "s_barrier\n"
    "s_lshl_b32 s56, s45, 1\n"
    "s_add_u32 s56, s56, s59\n"
    "s_add_u32 s52, s45, 2\n"
    "s_and_b32 s52, s52, 3\n"
    "s_mul_i32 s52, s52, 24576\n"
    "s_add_u32 s52, s52, s62\n"
    "s_add_u32 s52, s52, 32768\n"
    "ds_read_b128 v[8:11], v123 offset:0\n"
    "ds_read_b128 v[12:15], v123 offset:16384\n"
    "v_add_u32 v6, s52, v0\n"
    "v_add_u32 v7, s62, v6\n"
    "s_lshl_b32 s57, s56, 13\n"
    "s_add_u32 s57, s57, s71\n"
    "s_add_u32 s57, s57, s62\n"
    "s_add_u32 s67, s57, 4096\n"
    "s_add_u32 s68, s57, s62\n"
    "s_add_u32 s68, s68, 0x80000\n"
    "s_add_u32 s48, s56, 1\n"
    "s_lshl_b32 s69, s44, 1\n"
    "s_add_u32 s69, s69, s59\n"
    "s_and_b32 s66, s69, 15\n"
    "s_lshl_b32 s66, s66, 1\n"
    "s_add_u32 s66, s66, s64\n"
    "s_lshl_b32 s66, s66, 14\n"
    "s_lshl_b32 s52, s63, 12\n"
    "s_add_u32 s66, s66, s52\n"
    "s_add_u32 s66, s66, s70\n"
    "s_add_u32 s66, s66, 0x40000\n"
    "s_cmp_lg_u32 s45, 1\n"
    "s_cbranch_scc1 LSH_NP_%=\n"
    "s_lshl_b32 s54, 2, 16\n"
    "s_lshl_b32 s52, s59, 15\n"
    "s_add_u32 s54, s54, s52\n"
    "s_add_u32 s54, s54, s65\n"
    "s_add_u32 s54, s54, 0x400000\n"
    "s_add_u32 s55, s54, 4096\n"
    "s_add_u32 m0, s65, 0\n"
    "s_nop 0\n"
    "buffer_load_dwordx4 v0, s[40:43], s54 offen offset:0 sc1 lds\n"
    "buffer_load_dwordx4 v0, s[40:43], s54 offen offset:1024 sc1 lds\n"
    "buffer_load_dwordx4 v0, s[40:43], s54 offen offset:2048 sc1 lds\n"
    "buffer_load_dwordx4 v0, s[40:43], s54 offen offset:3072 sc1 lds\n"
    "s_add_u32 m0, s65, 4096\n"
    "s_nop 0\n"
    "buffer_load_dwordx4 v0, s[40:43], s55 offen offset:0 sc1 lds\n"
    "buffer_load_dwordx4 v0, s[40:43], s55 offen offset:1024 sc1 lds\n"
    "buffer_load_dwordx4 v0, s[40:43], s55 offen offset:2048 sc1 lds\n"
    "buffer_load_dwordx4 v0, s[40:43], s55 offen offset:3072 sc1 lds\n"
    "LSH_NP_%=:\n"
    "s_waitcnt lgkmcnt(0)\n"
    "v_mfma_f32_16x16x32_bf16 v[24:27], a[0:3], v[8:11], 0\n"
    "ds_read_b128 v[16:19], v123 offset:1024\n"
    "v_mfma_f32_16x16x32_bf16 v[32:35], a[0:3], v[12:15], 0\n"
    "ds_read_b128 v[20:23], v123 offset:17408\n"
    "v_mfma_f32_16x16x32_bf16 v[28:31], a[64:67], v[8:11], 0\n"
    "v_mfma_f32_16x16x32_bf16 v[36:39], a[64:67], v[12:15], 0\n"
    "v_mfma_f32_16x16x32_bf16 v[40:43], v[8:11], v[128:131], 0\n"
    "v_mfma_f32_16x16x32_bf16 v[44:47], v[12:15], v[128:131], 0\n"
    "v_mfma_f32_16x16x32_bf16 v[48:51], v[8:11], v[192:195], 0\n"
    "v_mfma_f32_16x16x32_bf16 v[52:55], v[12:15], v[192:195], 0\n"
    "s_waitcnt lgkmcnt(0)\n"
    "v_mfma_f32_16x16x32_bf16 v[24:27], a[4:7], v[16:19], v[24:27]\n"
    "ds_read_b128 v[8:11], v123 offset:2048\n"
    "v_mfma_f32_16x16x32_bf16 v[32:35], a[4:7], v[20:23], v[32:35]\n"
    "ds_read_b128 v[12:15], v123 offset:18432\n"
    "v_mfma_f32_16x16x32_bf16 v[28:31], a[68:71], v[16:19], v[28:31]\n"
    "v_mfma_f32_16x16x32_bf16 v[36:39], a[68:71], v[20:23], v[36:39]\n"
    "v_mfma_f32_16x16x32_bf16 v[40:43], v[16:19], v[132:135], v[40:43]\n"
    "v_mfma_f32_16x16x32_bf16 v[44:47], v[20:23], v[132:135], v[44:47]\n"
    "v_mfma_f32_16x16x32_bf16 v[48:51], v[16:19], v[196:199], v[48:51]\n"
    "v_mfma_f32_16x16x32_bf16 v[52:55], v[20:23], v[196:199], v[52:55]\n"
    "s_waitcnt lgkmcnt(0)\n"
    "v_mfma_f32_16x16x32_bf16 v[24:27], a[8:11], v[8:11], v[24:27]\n"
    "ds_read_b128 v[16:19], v123 offset:3072\n"
    "v_mfma_f32_16x16x32_bf16 v[32:35], a[8:11], v[12:15], v[32:35]\n"
    "ds_read_b128 v[20:23], v123 offset:19456\n"
    "v_mfma_f32_16x16x32_bf16 v[28:31], a[72:75], v[8:11], v[28:31]\n"
    "v_mfma_f32_16x16x32_bf16 v[36:39], a[72:75], v[12:15], v[36:39]\n"
    "v_mfma_f32_16x16x32_bf16 v[40:43], v[8:11], v[136:139], v[40:43]\n"
    "v_mfma_f32_16x16x32_bf16 v[44:47], v[12:15], v[136:139], v[44:47]\n"
    "v_mfma_f32_16x16x32_bf16 v[48:51], v[8:11], v[200:203], v[48:51]\n"
    "v_mfma_f32_16x16x32_bf16 v[52:55], v[12:15], v[200:203], v[52:55]\n"
    "s_waitcnt lgkmcnt(0)\n"
    "v_mfma_f32_16x16x32_bf16 v[24:27], a[12:15], v[16:19], v[24:27]\n"
    "ds_read_b128 v[8:11], v123 offset:4096\n"
    "v_mfma_f32_16x16x32_bf16 v[32:35], a[12:15], v[20:23], v[32:35]\n"
    "ds_read_b128 v[12:15], v123 offset:20480\n"
    "v_mfma_f32_16x16x32_bf16 v[28:31], a[76:79], v[16:19], v[28:31]\n"
    "v_mfma_f32_16x16x32_bf16 v[36:39], a[76:79], v[20:23], v[36:39]\n"
    "v_mfma_f32_16x16x32_bf16 v[40:43], v[16:19], v[140:143], v[40:43]\n"
    "v_mfma_f32_16x16x32_bf16 v[44:47], v[20:23], v[140:143], v[44:47]\n"
    "v_mfma_f32_16x16x32_bf16 v[48:51], v[16:19], v[204:207], v[48:51]\n"
    "v_mfma_f32_16x16x32_bf16 v[52:55], v[20:23], v[204:207], v[52:55]\n"
    "s_waitcnt lgkmcnt(0)\n"
    "v_mfma_f32_16x16x32_bf16 v[24:27], a[16:19], v[8:11], v[24:27]\n"
    "ds_read_b128 v[16:19], v123 offset:5120\n"
    "v_mfma_f32_16x16x32_bf16 v[32:35], a[16:19], v[12:15], v[32:35]\n"
    "ds_read_b128 v[20:23], v123 offset:21504\n"
    "v_mfma_f32_16x16x32_bf16 v[28:31], a[80:83], v[8:11], v[28:31]\n"
    "v_mfma_f32_16x16x32_bf16 v[36:39], a[80:83], v[12:15], v[36:39]\n"
    "v_mfma_f32_16x16x32_bf16 v[40:43], v[8:11], v[144:147], v[40:43]\n"
    "v_mfma_f32_16x16x32_bf16 v[44:47], v[12:15], v[144:147], v[44:47]\n"
    "v_mfma_f32_16x16x32_bf16 v[48:51], v[8:11], v[208:211], v[48:51]\n"
    "v_mfma_f32_16x16x32_bf16 v[52:55], v[12:15], v[208:211], v[52:55]\n"
    "s_waitcnt lgkmcnt(0)\n"
    "v_mfma_f32_16x16x32_bf16 v[24:27], a[20:23], v[16:19], v[24:27]\n"
    "ds_read_b128 v[8:11], v123 offset:6144\n"
    "v_mfma_f32_16x16x32_bf16 v[32:35], a[20:23], v[20:23], v[32:35]\n"
    "ds_read_b128 v[12:15], v123 offset:22528\n"
    "v_mfma_f32_16x16x32_bf16 v[28:31], a[84:87], v[16:19], v[28:31]\n"
    "v_mfma_f32_16x16x32_bf16 v[36:39], a[84:87], v[20:23], v[36:39]\n"
    "v_mfma_f32_16x16x32_bf16 v[40:43], v[16:19], v[148:151], v[40:43]\n"
    "v_mfma_f32_16x16x32_bf16 v[44:47], v[20:23], v[148:151], v[44:47]\n"
    "v_mfma_f32_16x16x32_bf16 v[48:51], v[16:19], v[212:215], v[48:51]\n"
    "v_mfma_f32_16x16x32_bf16 v[52:55], v[20:23], v[212:215], v[52:55]\n"
    "s_waitcnt lgkmcnt(0)\n"
    "v_mfma_f32_16x16x32_bf16 v[24:27], a[24:27], v[8:11], v[24:27]\n"
    "ds_read_b128 v[16:19], v123 offset:7168\n"
    "v_mfma_f32_16x16x32_bf16 v[32:35], a[24:27], v[12:15], v[32:35]\n"
    "ds_read_b128 v[20:23], v123 offset:23552\n"
    "v_mfma_f32_16x16x32_bf16 v[28:31], a[88:91], v[8:11], v[28:31]\n"
    "v_mfma_f32_16x16x32_bf16 v[36:39], a[88:91], v[12:15], v[36:39]\n"
    "v_mfma_f32_16x16x32_bf16 v[40:43], v[8:11], v[152:155], v[40:43]\n"
    "v_mfma_f32_16x16x32_bf16 v[44:47], v[12:15], v[152:155], v[44:47]\n"
    "v_mfma_f32_16x16x32_bf16 v[48:51], v[8:11], v[216:219], v[48:51]\n"
    "v_mfma_f32_16x16x32_bf16 v[52:55], v[12:15], v[216:219], v[52:55]\n"
    "s_waitcnt lgkmcnt(0)\n"
    "s_cmp_eq_u32 s46, 0\n"
    "s_cbranch_scc1 LSH_NOF_%=\n"
    "s_waitcnt vmcnt(0)\n"
    "s_add_u32 s52, s98, s46\n"
    "v_mov_b32 v120, s52\n"
    "s_mov_b64 exec, 0xffff\n"
    "buffer_store_dwordx2 v[120:121], v5, s[40:43], s96 offen sc1\n"
    "s_mov_b64 exec, -1\n"
    "s_mov_b32 s46, 0\n"
    "LSH_NOF_%=:\n"
    "v_mfma_f32_16x16x32_bf16 v[24:27], a[28:31], v[16:19], v[24:27]\n"
    "ds_read_b128 v[8:11], v123 offset:8192\n"
    "v_mfma_f32_16x16x32_bf16 v[32:35], a[28:31], v[20:23], v[32:35]\n"
    "ds_read_b128 v[12:15], v123 offset:24576\n"
    "v_mfma_f32_16x16x32_bf16 v[28:31], a[92:95], v[16:19], v[28:31]\n"
    "v_mfma_f32_16x16x32_bf16 v[36:39], a[92:95], v[20:23], v[36:39]\n"
    "v_mfma_f32_16x16x32_bf16 v[40:43], v[16:19], v[156:159], v[40:43]\n"
    "v_mfma_f32_16x16x32_bf16 v[44:47], v[20:23], v[156:159], v[44:47]\n"
    "v_mfma_f32_16x16x32_bf16 v[48:51], v[16:19], v[220:223], v[48:51]\n"
    "v_mfma_f32_16x16x32_bf16 v[52:55], v[20:23], v[220:223], v[52:55]\n"
    "s_waitcnt lgkmcnt(0)\n"
    "v_mfma_f32_16x16x32_bf16 v[24:27], a[32:35], v[8:11], v[24:27]\n"
    "ds_read_b128 v[16:19], v123 offset:9216\n"
    "v_mfma_f32_16x16x32_bf16 v[32:35], a[32:35], v[12:15], v[32:35]\n"
    "ds_read_b128 v[20:23], v123 offset:25600\n"
    "v_mfma_f32_16x16x32_bf16 v[28:31], a[96:99], v[8:11], v[28:31]\n"
    "v_mfma_f32_16x16x32_bf16 v[36:39], a[96:99], v[12:15], v[36:39]\n"
    "v_mfma_f32_16x16x32_bf16 v[40:43], v[8:11], v[160:163], v[40:43]\n"
    "v_mfma_f32_16x16x32_bf16 v[44:47], v[12:15], v[160:163], v[44:47]\n"
    "v_mfma_f32_16x16x32_bf16 v[48:51], v[8:11], v[224:227], v[48:51]\n"
    "v_mfma_f32_16x16x32_bf16 v[52:55], v[12:15], v[224:227], v[52:55]\n"
    "s_waitcnt lgkmcnt(0)\n"
    "v_mfma_f32_16x16x32_bf16 v[24:27], a[36:39], v[16:19], v[24:27]\n"
    "ds_read_b128 v[8:11], v123 offset:10240\n"
    "v_mfma_f32_16x16x32_bf16 v[32:35], a[36:39], v[20:23], v[32:35]\n"
    "ds_read_b128 v[12:15], v123 offset:26624\n"
    "v_mfma_f32_16x16x32_bf16 v[28:31], a[100:103], v[16:19], v[28:31]\n"
    "v_mfma_f32_16x16x32_bf16 v[36:39], a[100:103], v[20:23], v[36:39]\n"
    "v_mfma_f32_16x16x32_bf16 v[40:43], v[16:19], v[164:167], v[40:43]\n"
    "v_mfma_f32_16x16x32_bf16 v[44:47], v[20:23], v[164:167], v[44:47]\n"
    "v_mfma_f32_16x16x32_bf16 v[48:51], v[16:19], v[228:231], v[48:51]\n"
    "v_mfma_f32_16x16x32_bf16 v[52:55], v[20:23], v[228:231], v[52:55]\n"
    "s_waitcnt lgkmcnt(0)\n"
    "v_mfma_f32_16x16x32_bf16 v[24:27], a[40:43], v[8:11], v[24:27]\n"
    "ds_read_b128 v[16:19], v123 offset:11264\n"
    "v_mfma_f32_16x16x32_bf16 v[32:35], a[40:43], v[12:15], v[32:35]\n"
    "ds_read_b128 v[20:23], v123 offset:27648\n"
    "v_mfma_f32_16x16x32_bf16 v[28:31], a[104:107], v[8:11], v[28:31]\n"
    "v_mfma_f32_16x16x32_bf16 v[36:39], a[104:107], v[12:15], v[36:39]\n"
    "v_mfma_f32_16x16x32_bf16 v[40:43], v[8:11], v[168:171], v[40:43]\n"
    "v_mfma_f32_16x16x32_bf16 v[44:47], v[12:15], v[168:171], v[44:47]\n"
    "v_mfma_f32_16x16x32_bf16 v[48:51], v[8:11], v[232:235], v[48:51]\n"
    "v_mfma_f32_16x16x32_bf16 v[52:55], v[12:15], v[232:235], v[52:55]\n"
    "s_waitcnt lgkmcnt(0)\n"
    "v_mfma_f32_16x16x32_bf16 v[24:27], a[44:47], v[16:19], v[24:27]\n"
    "ds_read_b128 v[8:11], v123 offset:12288\n"
    "v_mfma_f32_16x16x32_bf16 v[32:35], a[44:47], v[20:23], v[32:35]\n"
    "ds_read_b128 v[12:15], v123 offset:28672\n"
    "v_mfma_f32_16x16x32_bf16 v[28:31], a[108:111], v[16:19], v[28:31]\n"
    "v_mfma_f32_16x16x32_bf16 v[36:39], a[108:111], v[20:23], v[36:39]\n"
    "v_mfma_f32_16x16x32_bf16 v[40:43], v[16:19], v[172:175], v[40:43]\n"
    "v_mfma_f32_16x16x32_bf16 v[44:47], v[20:23], v[172:175], v[44:47]\n"
    "v_mfma_f32_16x16x32_bf16 v[48:51], v[16:19], v[236:239], v[48:51]\n"
    "v_mfma_f32_16x16x32_bf16 v[52:55], v[20:23], v[236:239], v[52:55]\n"
    "s_waitcnt lgkmcnt(0)\n"
    "v_mfma_f32_16x16x32_bf16 v[24:27], a[48:51], v[8:11], v[24:27]\n"
    "ds_read_b128 v[16:19], v123 offset:13312\n"
    "v_mfma_f32_16x16x32_bf16 v[32:35], a[48:51], v[12:15], v[32:35]\n"
    "ds_read_b128 v[20:23], v123 offset:29696\n"
    "v_mfma_f32_16x16x32_bf16 v[28:31], a[112:115], v[8:11], v[28:31]\n"
    "v_mfma_f32_16x16x32_bf16 v[36:39], a[112:115], v[12:15], v[36:39]\n"
    "v_mfma_f32_16x16x32_bf16 v[40:43], v[8:11], v[176:179], v[40:43]\n"
    "v_mfma_f32_16x16x32_bf16 v[44:47], v[12:15], v[176:179], v[44:47]\n"
    "v_mfma_f32_16x16x32_bf16 v[48:51], v[8:11], v[240:243], v[48:51]\n"
    "v_mfma_f32_16x16x32_bf16 v[52:55], v[12:15], v[240:243], v[52:55]\n"
    "s_waitcnt lgkmcnt(0)\n"
    "v_mfma_f32_16x16x32_bf16 v[24:27], a[52:55], v[16:19], v[24:27]\n"
    "ds_read_b128 v[8:11], v123 offset:14336\n"
    "v_mfma_f32_16x16x32_bf16 v[32:35], a[52:55], v[20:23], v[32:35]\n"
    "ds_read_b128 v[12:15], v123 offset:30720\n"
    "v_mfma_f32_16x16x32_bf16 v[28:31], a[116:119], v[16:19], v[28:31]\n"
    "v_mfma_f32_16x16x32_bf16 v[36:39], a[116:119], v[20:23], v[36:39]\n"
    "v_mfma_f32_16x16x32_bf16 v[40:43], v[16:19], v[180:183], v[40:43]\n"
    "v_mfma_f32_16x16x32_bf16 v[44:47], v[20:23], v[180:183], v[44:47]\n"
    "v_mfma_f32_16x16x32_bf16 v[48:51], v[16:19], v[244:247], v[48:51]\n"
    "v_mfma_f32_16x16x32_bf16 v[52:55], v[20:23], v[244:247], v[52:55]\n"
    "s_waitcnt lgkmcnt(0)\n"
    "v_mfma_f32_16x16x32_bf16 v[24:27], a[56:59], v[8:11], v[24:27]\n"
    "ds_read_b128 v[16:19], v123 offset:15360\n"
    "v_mfma_f32_16x16x32_bf16 v[32:35], a[56:59], v[12:15], v[32:35]\n"
    "ds_read_b128 v[20:23], v123 offset:31744\n"
    "v_mfma_f32_16x16x32_bf16 v[28:31], a[120:123], v[8:11], v[28:31]\n"
    "v_mfma_f32_16x16x32_bf16 v[36:39], a[120:123], v[12:15], v[36:39]\n"
    "v_mfma_f32_16x16x32_bf16 v[40:43], v[8:11], v[184:187], v[40:43]\n"
    "v_mfma_f32_16x16x32_bf16 v[44:47], v[12:15], v[184:187], v[44:47]\n"
    "v_mfma_f32_16x16x32_bf16 v[48:51], v[8:11], v[248:251], v[48:51]\n"
    "v_mfma_f32_16x16x32_bf16 v[52:55], v[12:15], v[248:251], v[52:55]\n"
    "s_waitcnt lgkmcnt(0)\n"
    "v_mfma_f32_16x16x32_bf16 v[24:27], a[60:63], v[16:19], v[24:27]\n"
    "ds_read_b128 v[8:11], v123 offset:0\n"
    "v_mfma_f32_16x16x32_bf16 v[32:35], a[60:63], v[20:23], v[32:35]\n"
    "ds_read_b128 v[12:15], v123 offset:16384\n"
    "v_mfma_f32_16x16x32_bf16 v[28:31], a[124:127], v[16:19], v[28:31]\n"
    "v_mfma_f32_16x16x32_bf16 v[36:39], a[124:127], v[20:23], v[36:39]\n"
    "v_mfma_f32_16x16x32_bf16 v[40:43], v[16:19], v[188:191], v[40:43]\n"
    "v_mfma_f32_16x16x32_bf16 v[44:47], v[20:23], v[188:191], v[44:47]\n"
    "v_mfma_f32_16x16x32_bf16 v[48:51], v[16:19], v[252:255], v[48:51]\n"
    "v_mfma_f32_16x16x32_bf16 v[52:55], v[20:23], v[252:255], v[52:55]\n"
    "s_waitcnt lgkmcnt(0)\n"
    "ds_read_b128 v[16:19], v123 offset:1024\n"
    "ds_read_b128 v[20:23], v123 offset:17408\n"
    "v_mfma_f32_16x16x32_bf16 v[56:59], a[128:131], v[8:11], 0\n"
    "ds_read_b128 v[104:107], v123 offset:2048\n"
    "v_mfma_f32_16x16x32_bf16 v[64:67], a[128:131], v[12:15], 0\n"
    "ds_read_b128 v[108:111], v123 offset:18432\n"
    "v_mfma_f32_16x16x32_bf16 v[60:63], a[192:195], v[8:11], 0\n"
    "v_mfma_f32_16x16x32_bf16 v[68:71], a[192:195], v[12:15], 0\n"
    "s_waitcnt lgkmcnt(2)\n"
    "v_mfma_f32_16x16x32_bf16 v[56:59], a[132:135], v[16:19], v[56:59]\n"
    "ds_read_b128 v[8:11], v123 offset:3072\n"
    "v_cvt_pk_bf16_f32 v72, v24, v25\n"
    "v_mfma_f32_16x16x32_bf16 v[64:67], a[132:135], v[20:23], v[64:67]\n"
    "ds_read_b128 v[12:15], v123 offset:19456\n"
    "v_cvt_pk_bf16_f32 v73, v26, v27\n"
    "v_mfma_f32_16x16x32_bf16 v[60:63], a[196:199], v[16:19], v[60:63]\n"
    "v_cvt_pk_bf16_f32 v74, v28, v29\n"
    "v_cvt_pk_bf16_f32 v75, v30, v31\n"
    "v_mfma_f32_16x16x32_bf16 v[68:71], a[196:199], v[20:23], v[68:71]\n"
    "buffer_store_dwordx4 v[72:75], v0, s[40:43], s57 offen sc1\n"
    "s_waitcnt lgkmcnt(2)\n"
    "v_mfma_f32_16x16x32_bf16 v[56:59], a[136:139], v[104:107], v[56:59]\n"
    "ds_read_b128 v[16:19], v123 offset:4096\n"
    "v_cvt_pk_bf16_f32 v76, v32, v33\n"
    "v_mfma_f32_16x16x32_bf16 v[64:67], a[136:139], v[108:111], v[64:67]\n"
    "ds_read_b128 v[20:23], v123 offset:20480\n"
    "v_cvt_pk_bf16_f32 v77, v34, v35\n"
    "v_mfma_f32_16x16x32_bf16 v[60:63], a[200:203], v[104:107], v[60:63]\n"
    "v_cvt_pk_bf16_f32 v78, v36, v37\n"
    "v_cvt_pk_bf16_f32 v79, v38, v39\n"
    "v_mfma_f32_16x16x32_bf16 v[68:71], a[200:203], v[108:111], v[68:71]\n"
    "buffer_store_dwordx4 v[76:79], v0, s[40:43], s67 offen sc1\n"
    "s_waitcnt lgkmcnt(2)\n"
    "v_mfma_f32_16x16x32_bf16 v[56:59], a[140:143], v[8:11], v[56:59]\n"
    "ds_read_b128 v[104:107], v123 offset:5120\n"
    "v_cvt_pk_bf16_f32 v80, v40, v41\n"
    "v_mfma_f32_16x16x32_bf16 v[64:67], a[140:143], v[12:15], v[64:67]\n"
    "ds_read_b128 v[108:111], v123 offset:21504\n"
    "v_cvt_pk_bf16_f32 v81, v42, v43\n"
    "v_mfma_f32_16x16x32_bf16 v[60:63], a[204:207], v[8:11], v[60:63]\n"
    "v_cvt_pk_bf16_f32 v82, v44, v45\n"
    "v_cvt_pk_bf16_f32 v83, v46, v47\n"
    "v_mfma_f32_16x16x32_bf16 v[68:71], a[204:207], v[12:15], v[68:71]\n"
    "buffer_store_dwordx4 v[80:83], v0, s[40:43], s68 offen sc1\n"
    "s_waitcnt lgkmcnt(2)\n"
    "v_mfma_f32_16x16x32_bf16 v[56:59], a[144:147], v[16:19], v[56:59]\n"
    "ds_read_b128 v[8:11], v123 offset:6144\n"
    "v_cvt_pk_bf16_f32 v84, v48, v49\n"
    "v_mfma_f32_16x16x32_bf16 v[64:67], a[144:147], v[20:23], v[64:67]\n"
    "ds_read_b128 v[12:15], v123 offset:22528\n"
    "v_cvt_pk_bf16_f32 v85, v50, v51\n"
    "v_mfma_f32_16x16x32_bf16 v[60:63], a[208:211], v[16:19], v[60:63]\n"
    "v_cvt_pk_bf16_f32 v86, v52, v53\n"
    "v_cvt_pk_bf16_f32 v87, v54, v55\n"
    "v_mfma_f32_16x16x32_bf16 v[68:71], a[208:211], v[20:23], v[68:71]\n"
    "buffer_store_dwordx4 v[84:87], v0, s[40:43], s68 offen offset:1024 sc1\n"
    "s_waitcnt lgkmcnt(2)\n"
    "v_mfma_f32_16x16x32_bf16 v[56:59], a[148:151], v[104:107], v[56:59]\n"
    "ds_read_b128 v[16:19], v123 offset:7168\n"
    "v_mfma_f32_16x16x32_bf16 v[64:67], a[148:151], v[108:111], v[64:67]\n"
    "ds_read_b128 v[20:23], v123 offset:23552\n"
    "v_mfma_f32_16x16x32_bf16 v[60:63], a[212:215], v[104:107], v[60:63]\n"
    "ds_write_b128 v6, v[72:75] offset:8192\n"
    "v_mfma_f32_16x16x32_bf16 v[68:71], a[212:215], v[108:111], v[68:71]\n"
    "ds_write_b128 v6, v[76:79] offset:12288\n"
    "s_waitcnt lgkmcnt(4)\n"
    "v_mfma_f32_16x16x32_bf16 v[56:59], a[152:155], v[8:11], v[56:59]\n"
    "ds_read_b128 v[104:107], v123 offset:8192\n"
    "v_mfma_f32_16x16x32_bf16 v[64:67], a[152:155], v[12:15], v[64:67]\n"
    "ds_read_b128 v[108:111], v123 offset:24576\n"
    "v_mfma_f32_16x16x32_bf16 v[60:63], a[216:219], v[8:11], v[60:63]\n"
    "ds_write_b128 v7, v[80:83] offset:16384\n"
    "v_mfma_f32_16x16x32_bf16 v[68:71], a[216:219], v[12:15], v[68:71]\n"
    "ds_write_b128 v7, v[84:87] offset:17408\n"
    "s_waitcnt lgkmcnt(4)\n"
    "v_mfma_f32_16x16x32_bf16 v[56:59], a[156:159], v[16:19], v[56:59]\n"
    "ds_read_b128 v[8:11], v123 offset:9216\n"
    "v_mfma_f32_16x16x32_bf16 v[64:67], a[156:159], v[20:23], v[64:67]\n"
    "ds_read_b128 v[12:15], v123 offset:25600\n"
    "v_mfma_f32_16x16x32_bf16 v[60:63], a[220:223], v[16:19], v[60:63]\n"
    "buffer_load_dwordx4 v[88:91], v0, s[40:43], s66 offen offset:0 sc1\n"
    "v_mfma_f32_16x16x32_bf16 v[68:71], a[220:223], v[20:23], v[68:71]\n"
    "buffer_load_dwordx4 v[92:95], v0, s[40:43], s66 offen offset:1024 sc1\n"
    "s_waitcnt lgkmcnt(2)\n"
    "v_mfma_f32_16x16x32_bf16 v[56:59], a[160:163], v[104:107], v[56:59]\n"
    "ds_read_b128 v[16:19], v123 offset:10240\n"
    "v_mfma_f32_16x16x32_bf16 v[64:67], a[160:163], v[108:111], v[64:67]\n"
    "ds_read_b128 v[20:23], v123 offset:26624\n"
    "v_mfma_f32_16x16x32_bf16 v[60:63], a[224:227], v[104:107], v[60:63]\n"
    "buffer_load_dwordx4 v[96:99], v0, s[40:43], s66 offen offset:2048 sc1\n"
    "v_mfma_f32_16x16x32_bf16 v[68:71], a[224:227], v[108:111], v[68:71]\n"
    "buffer_load_dwordx4 v[100:103], v0, s[40:43], s66 offen offset:3072 sc1\n"
    "s_waitcnt lgkmcnt(2)\n"
    "v_mfma_f32_16x16x32_bf16 v[56:59], a[164:167], v[8:11], v[56:59]\n"
    "ds_read_b128 v[104:107], v123 offset:11264\n"
    "v_mfma_f32_16x16x32_bf16 v[64:67], a[164:167], v[12:15], v[64:67]\n"
    "ds_read_b128 v[108:111], v123 offset:27648\n"
    "v_mfma_f32_16x16x32_bf16 v[60:63], a[228:231], v[8:11], v[60:63]\n"
    "v_mfma_f32_16x16x32_bf16 v[68:71], a[228:231], v[12:15], v[68:71]\n"
    "s_waitcnt lgkmcnt(2)\n"
    "v_mfma_f32_16x16x32_bf16 v[56:59], a[168:171], v[16:19], v[56:59]\n"
    "ds_read_b128 v[8:11], v123 offset:12288\n"
    "v_mfma_f32_16x16x32_bf16 v[64:67], a[168:171], v[20:23], v[64:67]\n"
    "ds_read_b128 v[12:15], v123 offset:28672\n"
    "v_mfma_f32_16x16x32_bf16 v[60:63], a[232:235], v[16:19], v[60:63]\n"
    "v_mfma_f32_16x16x32_bf16 v[68:71], a[232:235], v[20:23], v[68:71]\n"
    "s_waitcnt lgkmcnt(2)\n"
    "v_mfma_f32_16x16x32_bf16 v[56:59], a[172:175], v[104:107], v[56:59]\n"
    "ds_read_b128 v[16:19], v123 offset:13312\n"
    "v_mfma_f32_16x16x32_bf16 v[64:67], a[172:175], v[108:111], v[64:67]\n"
    "ds_read_b128 v[20:23], v123 offset:29696\n"
    "v_mfma_f32_16x16x32_bf16 v[60:63], a[236:239], v[104:107], v[60:63]\n"
    "v_mfma_f32_16x16x32_bf16 v[68:71], a[236:239], v[108:111], v[68:71]\n"
    "s_waitcnt lgkmcnt(2)\n"
    "v_mfma_f32_16x16x32_bf16 v[56:59], a[176:179], v[8:11], v[56:59]\n"
    "ds_read_b128 v[104:107], v123 offset:14336\n"
    "v_mfma_f32_16x16x32_bf16 v[64:67], a[176:179], v[12:15], v[64:67]\n"
    "ds_read_b128 v[108:111], v123 offset:30720\n"
    "v_mfma_f32_16x16x32_bf16 v[60:63], a[240:243], v[8:11], v[60:63]\n"
    "v_mfma_f32_16x16x32_bf16 v[68:71], a[240:243], v[12:15], v[68:71]\n"
    "s_waitcnt lgkmcnt(2)\n"
    "v_mfma_f32_16x16x32_bf16 v[56:59], a[180:183], v[16:19], v[56:59]\n"
    "ds_read_b128 v[8:11], v123 offset:15360\n"
    "v_mfma_f32_16x16x32_bf16 v[64:67], a[180:183], v[20:23], v[64:67]\n"
    "ds_read_b128 v[12:15], v123 offset:31744\n"
    "v_mfma_f32_16x16x32_bf16 v[60:63], a[244:247], v[16:19], v[60:63]\n"
    "v_mfma_f32_16x16x32_bf16 v[68:71], a[244:247], v[20:23], v[68:71]\n"
    "s_waitcnt lgkmcnt(2)\n"
    "v_mfma_f32_16x16x32_bf16 v[56:59], a[184:187], v[104:107], v[56:59]\n"
    "v_mfma_f32_16x16x32_bf16 v[64:67], a[184:187], v[108:111], v[64:67]\n"
    "v_mfma_f32_16x16x32_bf16 v[60:63], a[248:251], v[104:107], v[60:63]\n"
    "v_mfma_f32_16x16x32_bf16 v[68:71], a[248:251], v[108:111], v[68:71]\n"
    "s_waitcnt lgkmcnt(0)\n"
    "s_waitcnt vmcnt(4)\n"
    "s_cmp_eq_u32 s48, 0\n"
    "s_cbranch_scc1 LSH_KF2_%=\n"
    "s_add_u32 s52, s98, s48\n"
    "v_mov_b32 v120, s52\n"
    "s_mov_b64 exec, 0xffff\n"
    "buffer_store_dwordx2 v[120:121], v5, s[40:43], s97 offen sc1\n"
    "s_mov_b64 exec, -1\n"
    "s_mov_b32 s48, 0\n"
    "LSH_KF2_%=:\n"
    "v_mfma_f32_16x16x32_bf16 v[56:59], a[188:191], v[8:11], v[56:59]\n"
    "v_mfma_f32_16x16x32_bf16 v[64:67], a[188:191], v[12:15], v[64:67]\n"
    "v_mfma_f32_16x16x32_bf16 v[60:63], a[252:255], v[8:11], v[60:63]\n"
    "v_mfma_f32_16x16x32_bf16 v[68:71], a[252:255], v[12:15], v[68:71]\n"
    "s_add_u32 s45, s45, 1\n"
    "s_nop 3\n"
    "v_cvt_pk_bf16_f32 v72, v56, v57\n"
    "v_cvt_pk_bf16_f32 v73, v58, v59\n"
    "v_cvt_pk_bf16_f32 v74, v60, v61\n"
    "v_cvt_pk_bf16_f32 v75, v62, v63\n"
    "ds_write_b128 v6, v[72:75] offset:0\n"
    "v_cvt_pk_bf16_f32 v76, v64, v65\n"
    "v_cvt_pk_bf16_f32 v77, v66, v67\n"
    "v_cvt_pk_bf16_f32 v78, v68, v69\n"
    "v_cvt_pk_bf16_f32 v79, v70, v71\n"
    "ds_write_b128 v6, v[76:79] offset:4096\n"
    "s_branch LSH_LOOP_%=\n"
    "LSH_FAIL_%=:\n"
    "v_mov_b32 v16, 0x200\n"
    "s_mov_b32 s52, 0x40000\n"
    "buffer_store_dword v16, off, s[40:43], s52 sc1\n"
    "LSH_END_%=:\n"
    "s_waitcnt vmcnt(0) lgkmcnt(0)\n"
    "s_endpgm\n"
        :
        : "s"(p.ws), "s"(h), "s"(w), "s"(par), "s"(p.mem0), "s"(p.comm - 64 * MB + h * 2 * MB), "s"(p.Wq), "s"(p.Wk), "s"(p.Wv), "s"(p.comm), "s"(bid),
          "{v0}"(lane * 16), "{v1}"(bc ? 0xEEEE : 0x4444), "{v2}"(SH_KM + (4 * g + (c >> 2)) * 32 + (c & 3) * 8), "{v3}"(SH_KM + (32 * w + c) * 32 + g * 8),
          "{v4}"(scat), "{v5}"(SH_VM + lane * 8), "{v8}"(src), "{v9}"(rd), "{v10}"((w & 1) * 16 + c - 4 * g), "{v15}"((tc * DM + 128 * w + 16 * g + 2 * bc) * 2)
        : "memory", "vcc", "scc", "v6", "v7", "v11", "v14", CLOB_V, CLOB_A, CLOB_S);
    __builtin_unreachable();
  }
  if (idx >= 32) {
    k.lane = opaque_lane();
    const Params p = fresh_params(comm);
    const int lane = k.lane, c = lane & 15, g = lane >> 4;
    get_tag(k, tag_load(k));
    BAR();
    if (w == 3) set_flag(k, F_IN + bid * 128);
    if (w >= 2) {
      if (idx == 32 && w == 2) {
        unsigned t[2] = {~0u, ~0u}, acc = 0;
#pragma unroll 1
        for (int rep = 0; rep < 5; ++rep)
#pragma unroll
          for (int i = 0; i < 2; ++i) {
            const unsigned off = p.comm + 0x1f0000 + i * 8192;
            st16(k.rs, (u32x4){0u, 0u, 0u, 0u}, (lane & 7) * 16, off);
            DRAIN();
            const unsigned long long t0 = __builtin_amdgcn_s_memtime();
            asm volatile("s_waitcnt lgkmcnt(0)" ::: "memory");
            acc += __builtin_amdgcn_raw_buffer_load_b32(k.rs, 0, off, 16);
            asm volatile("s_waitcnt vmcnt(0)" ::"v"(acc) : "memory");
            t[i] = min(t[i], (unsigned)(__builtin_amdgcn_s_memtime() - t0));
          }
        k.tag[1] ^= t[1] < t[0] ? 0x80000000u : 0u;
        set_flag(k, F_CAL);
      }
      return;
    }
    const int r = (idx - 32) * 2 + w;
    const unsigned tq = lane & 7;
    const unsigned ooff = C_MEMX + r * 128 + (2 * (tq & 3) + (g & 1)) * 16 + (tq >> 2) * 8 + (((lane >> 3) & 1) + 2 * (g >> 1)) * 16384;
    const unsigned pin = C_PART + (r >> 1) * 16384 + (r & 1) * 4096 + lane * 16;
    const unsigned cb = wait_cal(k, p.comm, 0), fb = 2 * p.comm + 8192 - cb;
#pragma unroll
    for (int i = 0; i < 8; ++i) st16(k.rs, (u32x4){SENT, SENT, SENT, SENT}, pin + (i & 3) * 1024, cb + (i >> 2) * 524288);
#pragma unroll
    for (int i = 0; i < 2; ++i) st16(k.rs, (u32x4){0u, 0u, 0u, 0u}, lane * 16 + i * 1024, cb + C_FLAG + (r >> 2) * 16384 + (r & 3) * 2048);
#pragma unroll
    for (int i = 0; i < 4; ++i) __builtin_amdgcn_raw_buffer_store_b64((u32x2){SENT, SENT}, k.rs, ooff, cb + i * 65536, 16);
    DRAIN();
    set_flag(k, F_RDY + r * 128);
    const unsigned long long tag64 = (unsigned long long)k.tag[0] | ((unsigned long long)k.tag[1] << 32);
    u32x4 sel;
    {
      const int sg = 2 * (c & 3) + ((c >> 2) & 1);
#pragma unroll
      for (int j = 0; j < 4; ++j) sel[j] = (sg == 2 * j) ? 0x3f80u : ((sg == 2 * j + 1) ? 0x3f800000u : 0u);
    }
    asm volatile(
    "s_mov_b64 s[40:41], %0\n"
    "s_mov_b32 s42, 0x10000000\n"
    "s_mov_b32 s43, 0x00020000\n"
    "s_mov_b32 s59, %1\n"
    "s_mov_b32 s60, %2\n"
    "s_mov_b32 s61, %3\n"
    "s_mov_b32 s63, %4\n"
    "s_mov_b64 s[98:99], %5\n"
    "s_mov_b32 s71, %6\n"
    "s_mov_b32 s53, %6\n"
    "s_mov_b32 s44, 0\n"
    "s_mov_b32 s54, 0\n"
    "s_mov_b32 s55, 0x10000\n"
    "s_mov_b32 s56, 0x30000\n"
    "s_mov_b32 s46, 120\n"
    "s_mov_b32 s62, 120\n"
    "s_mov_b32 s47, 1\n"
    "v_mov_b32 v8, -1\n"
    "v_mov_b32 v9, -1\n"
    "v_mov_b32 v10, -1\n"
    "v_mov_b32 v11, -1\n"
    "v_mov_b32 v45, 0\n"
    "s_memrealtime s[50:51]\n"
    "s_waitcnt lgkmcnt(0)\n"
    "s_sub_u32 s50, s50, 3000\n"
    "s_mov_b32 s45, s50\n"
    "s_memrealtime s[68:69]\n"
    "s_memtime s[76:77]\n"
    "s_waitcnt lgkmcnt(0)\n"
    "s_mov_b32 s74, s68\n"
    "s_mov_b32 s73, s76\n"
    "s_mov_b32 s72, 0\n"
    "LRED_LOOP_%=:\n"
    "s_mov_b32 s67, 0\n"
    "s_mov_b32 s58, 0\n"
    "LRED_SCH_%=:\n"
    "buffer_load_dwordx4 v[12:15], off, s[40:43], s53 offset:1152 sc1\n"
    "s_add_u32 s58, s58, 1\n"
    "s_cmp_gt_u32 s58, 0x80000\n"
    "s_cbranch_scc1 LRED_FAIL_%=\n"
    "s_waitcnt vmcnt(0)\n"
    "s_memrealtime s[68:69]\n"
    "v_readfirstlane_b32 s66, v12\n"
    "v_readfirstlane_b32 s48, v14\n"
    "v_readfirstlane_b32 s49, v15\n"
    "s_nop 1\n"
    "s_sub_u32 s64, s66, s63\n"
    "s_cmp_lg_u64 s[48:49], s[98:99]\n"
    "s_waitcnt lgkmcnt(0)\n"
    "s_cbranch_scc1 LRED_INV_%=\n"
    "s_sub_u32 s52, s66, s45\n"
    "s_cmp_ge_u32 s52, 0x80000\n"
    "s_cbranch_scc1 LRED_NOW_%=\n"
    "s_sub_u32 s52, s64, s68\n"
    "s_sub_u32 s52, s52, 32\n"
    "s_cmp_ge_u32 s52, 0x10000\n"
    "s_cbranch_scc1 LRED_NEAR_%=\n"
    "LRED_INV_%=:\n"
    "s_sleep 1\n"
    "s_branch LRED_SCH_%=\n"
    "LRED_NOW_%=:\n"
    "s_mov_b32 s64, s68\n"
    "s_mov_b32 s52, 0\n"
    "s_and_b32 s57, s58, 0x3ff\n"
    "s_cmp_lg_u32 s57, 0\n"
    "s_cbranch_scc1 LRED_INV_%=\n"
    "LRED_NEAR_%=:\n"
    "s_cmp_lt_i32 s52, 0\n"
    "s_cselect_b32 s64, s64, s68\n"
    "s_cmp_lg_u32 s72, 0\n"
    "s_cbranch_scc1 LRED_PI_%=\n"
    "s_memrealtime s[68:69]\n"
    "s_memtime s[76:77]\n"
    "s_waitcnt lgkmcnt(0)\n"
    "s_sub_u32 s74, s68, s74\n"
    "s_sub_u32 s73, s76, s73\n"
    "v_cvt_f32_u32 v12, s74\n"
    "v_cvt_f32_u32 v13, s73\n"
    "v_rcp_f32 v12, v12\n"
    "v_mul_f32 v13, 0x43800000, v13\n"
    "v_mul_f32 v12, v12, v13\n"
    "v_cvt_u32_f32 v12, v12\n"
    "s_nop 0\n"
    "v_readfirstlane_b32 s72, v12\n"
    "s_nop 1\n"
    "s_max_u32 s72, s72, 4000\n"
    "s_min_u32 s72, s72, 6600\n"
    "s_mov_b32 s74, s68\n"
    "s_mov_b32 s73, s76\n"
    "LRED_PI_%=:\n"
    "s_sub_u32 s52, s64, s74\n"
    "s_mov_b32 s74, s64\n"
    "s_mul_i32 s52, s52, s72\n"
    "s_ashr_i32 s52, s52, 8\n"
    "s_add_u32 s73, s73, s52\n"
    "s_mov_b32 s75, s73\n"
    "LRED_TW_%=:\n"
    "s_memrealtime s[68:69]\n"
    "s_memtime s[76:77]\n"
    "s_waitcnt lgkmcnt(0)\n"
    "s_sub_u32 s52, s74, s68\n"
    "s_mul_i32 s52, s52, s72\n"
    "s_ashr_i32 s52, s52, 8\n"
    "s_sub_u32 s78, s75, s76\n"
    "s_min_i32 s78, s78, s52\n"
    "s_lshr_b32 s77, s72, 6\n"
    "s_sub_u32 s52, s52, s77\n"
    "s_max_i32 s78, s78, s52\n"
    "s_add_u32 s75, s78, s76\n"
    "s_cmp_gt_i32 s78, 190\n"
    "s_cbranch_scc1 LRED_TW_%=\n"
    "LRED_TF_%=:\n"
    "s_sub_u32 s78, s78, 32\n"
    "s_cmp_gt_i32 s78, 60\n"
    "s_cbranch_scc1 LRED_TF_%=\n"
    "LRED_LOAD_%=:\n"
    "buffer_load_dwordx4 v[16:19], v0, s[40:43], s54 offen offset:0 sc1\n"
    "buffer_load_dwordx4 v[20:23], v0, s[40:43], s54 offen offset:1024 sc1\n"
    "buffer_load_dwordx4 v[24:27], v0, s[40:43], s54 offen offset:2048 sc1\n"
    "buffer_load_dwordx4 v[28:31], v0, s[40:43], s54 offen offset:3072 sc1\n"
    "s_sub_u32 s52, s75, s73\n"
    "s_max_i32 s52, s52, -64\n"
    "s_min_i32 s52, s52, 64\n"
    "s_mul_i32 s52, s52, 3\n"
    "s_ashr_i32 s52, s52, 3\n"
    "s_add_u32 s72, s72, s52\n"
    "s_mov_b32 s73, s75\n"
    "s_waitcnt vmcnt(3)\n"
    "v_mfma_f32_16x16x32_bf16 v[32:35], v[4:7], v[16:19], 0\n"
    "v_max_u32 v14, v16, v18\n"
    "s_waitcnt vmcnt(2)\n"
    "v_mfma_f32_16x16x32_bf16 v[32:35], v[4:7], v[20:23], v[32:35]\n"
    "v_max3_u32 v14, v14, v20, v22\n"
    "s_waitcnt vmcnt(1)\n"
    "v_mfma_f32_16x16x32_bf16 v[32:35], v[4:7], v[24:27], v[32:35]\n"
    "v_max3_u32 v14, v14, v24, v26\n"
    "s_waitcnt vmcnt(0)\n"
    "v_mfma_f32_16x16x32_bf16 v[32:35], v[4:7], v[28:31], v[32:35]\n"
    "v_max3_u32 v14, v14, v28, v30\n"
    "v_cmp_eq_u32 vcc, -1, v14\n"
    "s_nop 1\n"
    "s_cbranch_vccnz LRED_RETRY_%=\n"
    "s_nop 2\n"
    "v_add_f32_dpp v36, v32, v32 row_ror:8 row_mask:0xf bank_mask:0xf\n"
    "v_add_f32_dpp v37, v33, v33 row_ror:8 row_mask:0xf bank_mask:0xf\n"
    "v_add_f32_dpp v38, v34, v34 row_ror:8 row_mask:0xf bank_mask:0xf\n"
    "v_add_f32_dpp v39, v35, v35 row_ror:8 row_mask:0xf bank_mask:0xf\n"
    "v_cvt_pk_bf16_f32 v36, v36, v37\n"
    "v_cvt_pk_bf16_f32 v37, v38, v39\n"
    "buffer_store_dwordx2 v[36:37], v1, s[40:43], s55 offen sc1\n"
    "s_memtime s[80:81]\n"
    "v_mov_b32 v40, v36\n"
    "v_mov_b32 v42, v37\n"
    "s_add_u32 s57, s98, s44\n"
    "s_add_u32 s57, s57, 1\n"
    "v_mov_b32 v41, s57\n"
    "v_mov_b32 v43, s57\n"
    "s_add_u32 s52, s44, 1\n"
    "s_and_b32 s52, s52, 15\n"
    "s_lshl_b32 s52, s52, 15\n"
    "s_mov_b64 exec, 0x00ff00ff\n"
    "buffer_store_dwordx4 v[40:43], v2, s[40:43], s52 offen sc1\n"
    "s_mov_b64 exec, -1\n"
    "s_add_u32 s44, s44, 1\n"
    "s_lshr_b32 s52, s44, 2\n"
    "s_lshl_b32 s65, s52, 14\n"
    "s_and_b32 s52, s44, 3\n"
    "s_lshl_b32 s52, s52, 11\n"
    "s_add_u32 s65, s65, s52\n"
    "s_add_u32 s65, s65, s71\n"
    "s_waitcnt lgkmcnt(0)\n"
    "s_sleep 12\n"
    "s_lshl_b32 s52, s44, 13\n"
    "buffer_load_dword v45, v3, s[40:43], s52 offen sc1\n"
    "s_sleep 8\n"
    "buffer_store_dwordx4 v[8:11], v0, s[40:43], s54 offen offset:0 sc1\n"
    "buffer_store_dwordx4 v[8:11], v0, s[40:43], s54 offen offset:1024 sc1\n"
    "buffer_store_dwordx4 v[8:11], v0, s[40:43], s54 offen offset:2048 sc1\n"
    "buffer_store_dwordx4 v[8:11], v0, s[40:43], s54 offen offset:3072 sc1\n"
    "buffer_store_dwordx2 v[8:9], v1, s[40:43], s56 offen sc1\n"
    "s_mov_b32 s53, s65\n"
    "s_xor_b32 s54, s54, 0x80000\n"
    "s_add_u32 s55, s55, 0x10000\n"
    "s_and_b32 s55, s55, 0x30000\n"
    "s_add_u32 s56, s56, 0x10000\n"
    "s_and_b32 s56, s56, 0x30000\n"
    "s_cmp_lt_u32 s44, 63\n"
    "s_cbranch_scc1 LRED_LOOP_%=\n"
    "s_branch LRED_END_%=\n"
    "LRED_RETRY_%=:\n"
    "s_nop 7\n"
    "s_add_u32 s67, s67, 1\n"
    "s_branch LRED_SCH_%=\n"
    "LRED_FAIL_%=:\n"
    "v_mov_b32 v12, 2\n"
    "s_mov_b32 s52, 0x40000\n"
    "buffer_store_dword v12, off, s[40:43], s52 sc1\n"
    "LRED_END_%=:\n"
    "s_waitcnt vmcnt(0)\n"
    "s_endpgm\n"
        :
        : "s"(p.ws), "s"(p.V1), "s"(p.V2 - p.EA), "s"(r), "s"((r & 8) ? 0 : p.EA), "s"(tag64), "s"(cb + C_FLAG), "{v0}"(cb + pin), "{v1}"(cb + ooff),
          "{v2}"(fb + P_ARC + (r >> 5) * 16384 + (r & 31) * 256 + (tq + 8 * (g & 1)) * 16), "{v3}"(p.comm - 64 * MB + (idx - 32) * 2 * MB + (w ? HP_V : HP_K) + 8192 + lane * 128), "{v[4:7]}"(sel), "{v44}"(lane * 32)
        : "memory", "vcc", "scc", CLOB_R, CLOB_S);
    __builtin_unreachable();
  }
  const int h = idx;
  const Params p = fresh_params(comm);
  const int lane = opaque_lane(), c = lane & 15, g = lane >> 4;
  LAS char* L = k.L;
  *(LAS u32x4*)(L + L_KM + w * 2048 + lane * 16) = (u32x4){0, 0, 0, 0};
  *(LAS u32x4*)(L + L_KM + w * 2048 + 1024 + lane * 16) = (u32x4){0, 0, 0, 0};
  const int tc = 2 * (c >> 2) + (c & 1), bc = (c >> 1) & 1;
  const unsigned sidx = bc ? 0xEEEE : 0x4444;
  const unsigned poff = (8 * w + (c >> 2)) * 16384 + ((c >> 1) & 1) * 4096 + h * 128 + ((c & 1) * 4 + g) * 16;
  const unsigned trq = L_Q + (4 * g + (c >> 2)) * 16 + (c & 1) * 8;
  const unsigned trk = L_KM + (4 * g + (c >> 2)) * 40 + (c & 3) * 8;
  const unsigned qwr = L_Q + (32 * w + c) * 16 + g * 4, kwr = L_KM + (32 * w + c) * 40 + g * 8;
  unsigned owr, sqkv, swo, rqkv, rwo;
  {
    const int t = lane & 7, T = (lane >> 3) & 1, grp = 4 * (w & 1) + 2 * T + (g >> 1);
    owr = L_O + (w >> 1) * 1024 + (16 * (grp >> 1) + 4 * (t >> 1) + 2 * (grp & 1) + (t & 1)) * 16 + (g & 1) * 8;
    const int dq = lane >> 4, pos = lane & 15, q = (lane >> 2) & 3, pp = lane & 3;
    sqkv = (16 * (dq & 1) + 2 * (dq >> 1) + 4 * (w >> 1) + (w & 1)) * 8192 + (pos ^ (2 * (dq + 4 * (w & 1)))) * 16;
    swo = (8 * (dq >> 1) + (dq & 1)) * 1024 + (pos ^ (2 * dq)) * 16;
    const int X = 2 * (q + 4 * (g & 1));
    rqkv = L_WO + g * 1024 + q * 256 + (((4 * w + (pp >> 1)) ^ X) * 16) + (pp & 1) * 8;
    rwo = L_WO + w * 32768 + g * 1024 + q * 256 + (((pp >> 1) ^ X) * 16) + (pp & 1) * 8;
  }
  const unsigned wob = L_WO + w * 32768 + lane * 16;
  asm volatile(
    "s_mov_b64 s[40:41], %0\n"
    "s_mov_b32 s42, 0x10000000\n"
    "s_mov_b32 s43, 0x00020000\n"
    "s_mov_b32 s60, %2\n"
    "s_mov_b32 s61, %1\n"
    "s_mov_b32 s59, %3\n"
    "s_mov_b32 s71, %5\n"
    "s_mov_b32 s70, %6\n"
    "s_mov_b32 s97, %11\n"
    "s_memrealtime s[92:93]\n"
    "s_waitcnt lgkmcnt(0)\n"
    "s_sub_u32 s92, s92, 3000\n"
    "s_mov_b32 s44, 0\n"
    "s_mov_b32 s46, 125\n"
    "s_mov_b32 s67, 0\n"
    "s_mov_b32 s58, 0\n"
    "s_lshl_b32 s62, s60, 11\n"
    "s_lshl_b32 s63, s61, 7\n"
    "s_mov_b32 s64, 0xff00ff00\n"
    "s_mov_b32 s65, 0xff00ff00\n"
    "s_mov_b32 s74, 0x400000\n"
    "s_mov_b32 s75, 0x00020000\n"
    "s_lshl_b32 s47, s61, 8\n"
    "s_lshl_b32 s48, s61, 17\n"
    "s_lshl_b32 s52, s60, 8\n"
    "s_add_u32 s48, s48, s52\n"
    "s_lshl_b32 s49, s60, 10\n"
    "s_add_u32 s49, s49, 32768\n"
    "s_lshl_b32 s50, s60, 15\n"
    "s_add_u32 s50, s50, 32768\n"
    "v_xor_b32 v12, 32, v11\n"
    "v_xor_b32 v13, 0x80, v10\n"
    "s_memrealtime s[68:69]\n"
    "s_memtime s[86:87]\n"
    "s_waitcnt lgkmcnt(0)\n"
    "s_mov_b32 s84, s68\n"
    "s_mov_b32 s83, s86\n"
    "buffer_load_dwordx2 v[56:57], off, s[40:43], 0 sc1\n"
    "buffer_load_dword v59, off, s[40:43], s71\n"
    "s_lshl_b32 s52, s61, 21\n"
    "s_add_u32 s52, s52, s71\n"
    "s_sub_u32 s52, s52, 0x4000000\n"
    "buffer_load_dword v59, off, s[40:43], s52\n"
    "s_mov_b64 s[76:77], %4\n"
    "s_mov_b32 s78, 8192\n"
    "s_mov_b32 s79, 0x00020000\n"
    "buffer_load_dword v60, v15, s[76:79], 0 offen offset:0\n"
    "buffer_load_dword v61, v15, s[76:79], 0 offen offset:32\n"
    "buffer_load_dword v62, v15, s[76:79], 0 offen offset:64\n"
    "buffer_load_dword v63, v15, s[76:79], 0 offen offset:96\n"
    "buffer_load_dword v64, v15, s[76:79], 0 offen offset:128\n"
    "buffer_load_dword v65, v15, s[76:79], 0 offen offset:160\n"
    "buffer_load_dword v66, v15, s[76:79], 0 offen offset:192\n"
    "buffer_load_dword v67, v15, s[76:79], 0 offen offset:224\n"
    "s_mov_b64 s[72:73], %8\n"
    "s_add_u32 m0, s49, 0\n"
    "s_add_u32 s52, s47, 0x0\n"
    "buffer_load_dwordx4 v9, s[72:75], s52 offen lds\n"
    "s_add_u32 m0, m0, 4096\n"
    "s_add_u32 s52, s47, 0x40000\n"
    "buffer_load_dwordx4 v9, s[72:75], s52 offen lds\n"
    "s_add_u32 m0, m0, 4096\n"
    "s_add_u32 s52, s47, 0x10000\n"
    "buffer_load_dwordx4 v9, s[72:75], s52 offen lds\n"
    "s_add_u32 m0, m0, 4096\n"
    "s_add_u32 s52, s47, 0x50000\n"
    "buffer_load_dwordx4 v9, s[72:75], s52 offen lds\n"
    "s_add_u32 m0, m0, 4096\n"
    "s_add_u32 s52, s47, 0x80000\n"
    "buffer_load_dwordx4 v9, s[72:75], s52 offen lds\n"
    "s_add_u32 m0, m0, 4096\n"
    "s_add_u32 s52, s47, 0xc0000\n"
    "buffer_load_dwordx4 v9, s[72:75], s52 offen lds\n"
    "s_add_u32 m0, m0, 4096\n"
    "s_add_u32 s52, s47, 0x90000\n"
    "buffer_load_dwordx4 v9, s[72:75], s52 offen lds\n"
    "s_add_u32 m0, m0, 4096\n"
    "s_add_u32 s52, s47, 0xd0000\n"
    "buffer_load_dwordx4 v9, s[72:75], s52 offen lds\n"
    "s_add_u32 m0, m0, 4096\n"
    "s_add_u32 s52, s47, 0x100000\n"
    "buffer_load_dwordx4 v9, s[72:75], s52 offen lds\n"
    "s_add_u32 m0, m0, 4096\n"
    "s_add_u32 s52, s47, 0x140000\n"
    "buffer_load_dwordx4 v9, s[72:75], s52 offen lds\n"
    "s_add_u32 m0, m0, 4096\n"
    "s_add_u32 s52, s47, 0x110000\n"
    "buffer_load_dwordx4 v9, s[72:75], s52 offen lds\n"
    "s_add_u32 m0, m0, 4096\n"
    "s_add_u32 s52, s47, 0x150000\n"
    "buffer_load_dwordx4 v9, s[72:75], s52 offen lds\n"
    "s_add_u32 m0, m0, 4096\n"
    "s_add_u32 s52, s47, 0x180000\n"
    "buffer_load_dwordx4 v9, s[72:75], s52 offen lds\n"
    "s_add_u32 m0, m0, 4096\n"
    "s_add_u32 s52, s47, 0x1c0000\n"
    "buffer_load_dwordx4 v9, s[72:75], s52 offen lds\n"
    "s_add_u32 m0, m0, 4096\n"
    "s_add_u32 s52, s47, 0x190000\n"
    "buffer_load_dwordx4 v9, s[72:75], s52 offen lds\n"
    "s_add_u32 m0, m0, 4096\n"
    "s_add_u32 s52, s47, 0x1d0000\n"
    "buffer_load_dwordx4 v9, s[72:75], s52 offen lds\n"
    "s_mov_b64 s[72:73], %8\n"
    "s_add_u32 m0, s49, 65536\n"
    "s_add_u32 s52, s47, 0x200000\n"
    "buffer_load_dwordx4 v9, s[72:75], s52 offen lds\n"
    "s_add_u32 m0, m0, 4096\n"
    "s_add_u32 s52, s47, 0x240000\n"
    "buffer_load_dwordx4 v9, s[72:75], s52 offen lds\n"
    "s_add_u32 m0, m0, 4096\n"
    "s_add_u32 s52, s47, 0x210000\n"
    "buffer_load_dwordx4 v9, s[72:75], s52 offen lds\n"
    "s_add_u32 m0, m0, 4096\n"
    "s_add_u32 s52, s47, 0x250000\n"
    "buffer_load_dwordx4 v9, s[72:75], s52 offen lds\n"
    "s_add_u32 m0, m0, 4096\n"
    "s_add_u32 s52, s47, 0x280000\n"
    "buffer_load_dwordx4 v9, s[72:75], s52 offen lds\n"
    "s_add_u32 m0, m0, 4096\n"
    "s_add_u32 s52, s47, 0x2c0000\n"
    "buffer_load_dwordx4 v9, s[72:75], s52 offen lds\n"
    "s_add_u32 m0, m0, 4096\n"
    "s_add_u32 s52, s47, 0x290000\n"
    "buffer_load_dwordx4 v9, s[72:75], s52 offen lds\n"
    "s_add_u32 m0, m0, 4096\n"
    "s_add_u32 s52, s47, 0x2d0000\n"
    "buffer_load_dwordx4 v9, s[72:75], s52 offen lds\n"
    "s_add_u32 m0, m0, 4096\n"
    "s_add_u32 s52, s47, 0x300000\n"
    "buffer_load_dwordx4 v9, s[72:75], s52 offen lds\n"
    "s_add_u32 m0, m0, 4096\n"
    "s_add_u32 s52, s47, 0x340000\n"
    "buffer_load_dwordx4 v9, s[72:75], s52 offen lds\n"
    "s_add_u32 m0, m0, 4096\n"
    "s_add_u32 s52, s47, 0x310000\n"
    "buffer_load_dwordx4 v9, s[72:75], s52 offen lds\n"
    "s_add_u32 m0, m0, 4096\n"
    "s_add_u32 s52, s47, 0x350000\n"
    "buffer_load_dwordx4 v9, s[72:75], s52 offen lds\n"
    "s_add_u32 m0, m0, 4096\n"
    "s_add_u32 s52, s47, 0x380000\n"
    "buffer_load_dwordx4 v9, s[72:75], s52 offen lds\n"
    "s_add_u32 m0, m0, 4096\n"
    "s_add_u32 s52, s47, 0x3c0000\n"
    "buffer_load_dwordx4 v9, s[72:75], s52 offen lds\n"
    "s_add_u32 m0, m0, 4096\n"
    "s_add_u32 s52, s47, 0x390000\n"
    "buffer_load_dwordx4 v9, s[72:75], s52 offen lds\n"
    "s_add_u32 m0, m0, 4096\n"
    "s_add_u32 s52, s47, 0x3d0000\n"
    "buffer_load_dwordx4 v9, s[72:75], s52 offen lds\n"
    "s_waitcnt vmcnt(16)\n"
    "v_readfirstlane_b32 s98, v56\n"
    "v_readfirstlane_b32 s99, v57\n"
    "s_nop 1\n"
    "s_add_u32 s98, s98, 0x9e3779b1\n"
    "s_add_u32 s99, s99, 0x00010003\n"
    "s_mov_b32 s52, 0x05040100\n"
    "s_mov_b32 s57, 0x07060302\n"
    "v_add_u32 v58, s62, v0\n"
    "v_perm_b32 v16, v61, v60, s52\n"
    "v_perm_b32 v17, v63, v62, s52\n"
    "v_perm_b32 v18, v61, v60, s57\n"
    "v_perm_b32 v19, v63, v62, s57\n"
    "v_min_u32 v16, -2, v16\n"
    "v_min_u32 v18, -2, v18\n"
    "ds_write_b128 v58, v[16:19] offset:0\n"
    "s_waitcnt lgkmcnt(0)\n"
    "v_perm_b32 v16, v65, v64, s52\n"
    "v_perm_b32 v17, v67, v66, s52\n"
    "v_perm_b32 v18, v65, v64, s57\n"
    "v_perm_b32 v19, v67, v66, s57\n"
    "v_min_u32 v16, -2, v16\n"
    "v_min_u32 v18, -2, v18\n"
    "ds_write_b128 v58, v[16:19] offset:1024\n"
    "s_waitcnt lgkmcnt(0)\n"
    "s_barrier\n"
    "ds_read_b64_tr_b16 a[128:129], v11 offset:0\n"
    "ds_read_b64_tr_b16 a[192:193], v12 offset:0\n"
    "ds_read_b64_tr_b16 a[130:131], v11 offset:4096\n"
    "ds_read_b64_tr_b16 a[194:195], v12 offset:4096\n"
    "ds_read_b64_tr_b16 a[132:133], v11 offset:8192\n"
    "ds_read_b64_tr_b16 a[196:197], v12 offset:8192\n"
    "ds_read_b64_tr_b16 a[134:135], v11 offset:12288\n"
    "ds_read_b64_tr_b16 a[198:199], v12 offset:12288\n"
    "ds_read_b64_tr_b16 a[136:137], v11 offset:16384\n"
    "ds_read_b64_tr_b16 a[200:201], v12 offset:16384\n"
    "ds_read_b64_tr_b16 a[138:139], v11 offset:20480\n"
    "ds_read_b64_tr_b16 a[202:203], v12 offset:20480\n"
    "ds_read_b64_tr_b16 a[140:141], v11 offset:24576\n"
    "ds_read_b64_tr_b16 a[204:205], v12 offset:24576\n"
    "ds_read_b64_tr_b16 a[142:143], v11 offset:28672\n"
    "ds_read_b64_tr_b16 a[206:207], v12 offset:28672\n"
    "ds_read_b64_tr_b16 a[144:145], v11 offset:32768\n"
    "ds_read_b64_tr_b16 a[208:209], v12 offset:32768\n"
    "ds_read_b64_tr_b16 a[146:147], v11 offset:36864\n"
    "ds_read_b64_tr_b16 a[210:211], v12 offset:36864\n"
    "ds_read_b64_tr_b16 a[148:149], v11 offset:40960\n"
    "ds_read_b64_tr_b16 a[212:213], v12 offset:40960\n"
    "ds_read_b64_tr_b16 a[150:151], v11 offset:45056\n"
    "ds_read_b64_tr_b16 a[214:215], v12 offset:45056\n"
    "ds_read_b64_tr_b16 a[152:153], v11 offset:49152\n"
    "ds_read_b64_tr_b16 a[216:217], v12 offset:49152\n"
    "ds_read_b64_tr_b16 a[154:155], v11 offset:53248\n"
    "ds_read_b64_tr_b16 a[218:219], v12 offset:53248\n"
    "ds_read_b64_tr_b16 a[156:157], v11 offset:57344\n"
    "ds_read_b64_tr_b16 a[220:221], v12 offset:57344\n"
    "ds_read_b64_tr_b16 a[158:159], v11 offset:61440\n"
    "ds_read_b64_tr_b16 a[222:223], v12 offset:61440\n"
    "s_waitcnt lgkmcnt(0)\n"
    "s_barrier\n"
    "s_mov_b64 s[72:73], %9\n"
    "s_add_u32 m0, s49, 0\n"
    "s_add_u32 s52, s47, 0x0\n"
    "buffer_load_dwordx4 v9, s[72:75], s52 offen lds\n"
    "s_add_u32 m0, m0, 4096\n"
    "s_add_u32 s52, s47, 0x40000\n"
    "buffer_load_dwordx4 v9, s[72:75], s52 offen lds\n"
    "s_add_u32 m0, m0, 4096\n"
    "s_add_u32 s52, s47, 0x10000\n"
    "buffer_load_dwordx4 v9, s[72:75], s52 offen lds\n"
    "s_add_u32 m0, m0, 4096\n"
    "s_add_u32 s52, s47, 0x50000\n"
    "buffer_load_dwordx4 v9, s[72:75], s52 offen lds\n"
    "s_add_u32 m0, m0, 4096\n"
    "s_add_u32 s52, s47, 0x80000\n"
    "buffer_load_dwordx4 v9, s[72:75], s52 offen lds\n"
    "s_add_u32 m0, m0, 4096\n"
    "s_add_u32 s52, s47, 0xc0000\n"
    "buffer_load_dwordx4 v9, s[72:75], s52 offen lds\n"
    "s_add_u32 m0, m0, 4096\n"
    "s_add_u32 s52, s47, 0x90000\n"
    "buffer_load_dwordx4 v9, s[72:75], s52 offen lds\n"
    "s_add_u32 m0, m0, 4096\n"
    "s_add_u32 s52, s47, 0xd0000\n"
    "buffer_load_dwordx4 v9, s[72:75], s52 offen lds\n"
    "s_add_u32 m0, m0, 4096\n"
    "s_add_u32 s52, s47, 0x100000\n"
    "buffer_load_dwordx4 v9, s[72:75], s52 offen lds\n"
    "s_add_u32 m0, m0, 4096\n"
    "s_add_u32 s52, s47, 0x140000\n"
    "buffer_load_dwordx4 v9, s[72:75], s52 offen lds\n"
    "s_add_u32 m0, m0, 4096\n"
    "s_add_u32 s52, s47, 0x110000\n"
    "buffer_load_dwordx4 v9, s[72:75], s52 offen lds\n"
    "s_add_u32 m0, m0, 4096\n"
    "s_add_u32 s52, s47, 0x150000\n"
    "buffer_load_dwordx4 v9, s[72:75], s52 offen lds\n"
    "s_add_u32 m0, m0, 4096\n"
    "s_add_u32 s52, s47, 0x180000\n"
    "buffer_load_dwordx4 v9, s[72:75], s52 offen lds\n"
    "s_add_u32 m0, m0, 4096\n"
    "s_add_u32 s52, s47, 0x1c0000\n"
    "buffer_load_dwordx4 v9, s[72:75], s52 offen lds\n"
    "s_add_u32 m0, m0, 4096\n"
    "s_add_u32 s52, s47, 0x190000\n"
    "buffer_load_dwordx4 v9, s[72:75], s52 offen lds\n"
    "s_add_u32 m0, m0, 4096\n"
    "s_add_u32 s52, s47, 0x1d0000\n"
    "buffer_load_dwordx4 v9, s[72:75], s52 offen lds\n"
    "s_waitcnt vmcnt(16)\n"
    "s_barrier\n"
    "v_add_u32 v11, 0x10000, v11\n"
    "v_add_u32 v12, 0x10000, v12\n"
    "ds_read_b64_tr_b16 a[160:161], v11 offset:0\n"
    "ds_read_b64_tr_b16 a[224:225], v12 offset:0\n"
    "ds_read_b64_tr_b16 a[162:163], v11 offset:4096\n"
    "ds_read_b64_tr_b16 a[226:227], v12 offset:4096\n"
    "ds_read_b64_tr_b16 a[164:165], v11 offset:8192\n"
    "ds_read_b64_tr_b16 a[228:229], v12 offset:8192\n"
    "ds_read_b64_tr_b16 a[166:167], v11 offset:12288\n"
    "ds_read_b64_tr_b16 a[230:231], v12 offset:12288\n"
    "ds_read_b64_tr_b16 a[168:169], v11 offset:16384\n"
    "ds_read_b64_tr_b16 a[232:233], v12 offset:16384\n"
    "ds_read_b64_tr_b16 a[170:171], v11 offset:20480\n"
    "ds_read_b64_tr_b16 a[234:235], v12 offset:20480\n"
    "ds_read_b64_tr_b16 a[172:173], v11 offset:24576\n"
    "ds_read_b64_tr_b16 a[236:237], v12 offset:24576\n"
    "ds_read_b64_tr_b16 a[174:175], v11 offset:28672\n"
    "ds_read_b64_tr_b16 a[238:239], v12 offset:28672\n"
    "ds_read_b64_tr_b16 a[176:177], v11 offset:32768\n"
    "ds_read_b64_tr_b16 a[240:241], v12 offset:32768\n"
    "ds_read_b64_tr_b16 a[178:179], v11 offset:36864\n"
    "ds_read_b64_tr_b16 a[242:243], v12 offset:36864\n"
    "ds_read_b64_tr_b16 a[180:181], v11 offset:40960\n"
    "ds_read_b64_tr_b16 a[244:245], v12 offset:40960\n"
    "ds_read_b64_tr_b16 a[182:183], v11 offset:45056\n"
    "ds_read_b64_tr_b16 a[246:247], v12 offset:45056\n"
    "ds_read_b64_tr_b16 a[184:185], v11 offset:49152\n"
    "ds_read_b64_tr_b16 a[248:249], v12 offset:49152\n"
    "ds_read_b64_tr_b16 a[186:187], v11 offset:53248\n"
    "ds_read_b64_tr_b16 a[250:251], v12 offset:53248\n"
    "ds_read_b64_tr_b16 a[188:189], v11 offset:57344\n"
    "ds_read_b64_tr_b16 a[252:253], v12 offset:57344\n"
    "ds_read_b64_tr_b16 a[190:191], v11 offset:61440\n"
    "ds_read_b64_tr_b16 a[254:255], v12 offset:61440\n"
    "v_subrev_u32 v11, 0x10000, v11\n"
    "v_subrev_u32 v12, 0x10000, v12\n"
    "s_waitcnt lgkmcnt(0)\n"
    "s_barrier\n"
    "s_mov_b64 s[72:73], %9\n"
    "s_add_u32 m0, s49, 65536\n"
    "s_add_u32 s52, s47, 0x200000\n"
    "buffer_load_dwordx4 v9, s[72:75], s52 offen lds\n"
    "s_add_u32 m0, m0, 4096\n"
    "s_add_u32 s52, s47, 0x240000\n"
    "buffer_load_dwordx4 v9, s[72:75], s52 offen lds\n"
    "s_add_u32 m0, m0, 4096\n"
    "s_add_u32 s52, s47, 0x210000\n"
    "buffer_load_dwordx4 v9, s[72:75], s52 offen lds\n"
    "s_add_u32 m0, m0, 4096\n"
    "s_add_u32 s52, s47, 0x250000\n"
    "buffer_load_dwordx4 v9, s[72:75], s52 offen lds\n"
    "s_add_u32 m0, m0, 4096\n"
    "s_add_u32 s52, s47, 0x280000\n"
    "buffer_load_dwordx4 v9, s[72:75], s52 offen lds\n"
    "s_add_u32 m0, m0, 4096\n"
    "s_add_u32 s52, s47, 0x2c0000\n"
    "buffer_load_dwordx4 v9, s[72:75], s52 offen lds\n"
    "s_add_u32 m0, m0, 4096\n"
    "s_add_u32 s52, s47, 0x290000\n"
    "buffer_load_dwordx4 v9, s[72:75], s52 offen lds\n"
    "s_add_u32 m0, m0, 4096\n"
    "s_add_u32 s52, s47, 0x2d0000\n"
    "buffer_load_dwordx4 v9, s[72:75], s52 offen lds\n"
    "s_add_u32 m0, m0, 4096\n"
    "s_add_u32 s52, s47, 0x300000\n"
    "buffer_load_dwordx4 v9, s[72:75], s52 offen lds\n"
    "s_add_u32 m0, m0, 4096\n"
    "s_add_u32 s52, s47, 0x340000\n"
    "buffer_load_dwordx4 v9, s[72:75], s52 offen lds\n"
    "s_add_u32 m0, m0, 4096\n"
    "s_add_u32 s52, s47, 0x310000\n"
    "buffer_load_dwordx4 v9, s[72:75], s52 offen lds\n"
    "s_add_u32 m0, m0, 4096\n"
    "s_add_u32 s52, s47, 0x350000\n"
    "buffer_load_dwordx4 v9, s[72:75], s52 offen lds\n"
    "s_add_u32 m0, m0, 4096\n"
    "s_add_u32 s52, s47, 0x380000\n"
    "buffer_load_dwordx4 v9, s[72:75], s52 offen lds\n"
    "s_add_u32 m0, m0, 4096\n"
    "s_add_u32 s52, s47, 0x3c0000\n"
    "buffer_load_dwordx4 v9, s[72:75], s52 offen lds\n"
    "s_add_u32 m0, m0, 4096\n"
    "s_add_u32 s52, s47, 0x390000\n"
    "buffer_load_dwordx4 v9, s[72:75], s52 offen lds\n"
    "s_add_u32 m0, m0, 4096\n"
    "s_add_u32 s52, s47, 0x3d0000\n"
    "buffer_load_dwordx4 v9, s[72:75], s52 offen lds\n"
    "s_waitcnt vmcnt(16)\n"
    "s_barrier\n"
    "ds_read_b64_tr_b16 v[128:129], v11 offset:0\n"
    "ds_read_b64_tr_b16 v[192:193], v12 offset:0\n"
    "ds_read_b64_tr_b16 v[130:131], v11 offset:4096\n"
    "ds_read_b64_tr_b16 v[194:195], v12 offset:4096\n"
    "ds_read_b64_tr_b16 v[132:133], v11 offset:8192\n"
    "ds_read_b64_tr_b16 v[196:197], v12 offset:8192\n"
    "ds_read_b64_tr_b16 v[134:135], v11 offset:12288\n"
    "ds_read_b64_tr_b16 v[198:199], v12 offset:12288\n"
    "ds_read_b64_tr_b16 v[136:137], v11 offset:16384\n"
    "ds_read_b64_tr_b16 v[200:201], v12 offset:16384\n"
    "ds_read_b64_tr_b16 v[138:139], v11 offset:20480\n"
    "ds_read_b64_tr_b16 v[202:203], v12 offset:20480\n"
    "ds_read_b64_tr_b16 v[140:141], v11 offset:24576\n"
    "ds_read_b64_tr_b16 v[204:205], v12 offset:24576\n"
    "ds_read_b64_tr_b16 v[142:143], v11 offset:28672\n"
    "ds_read_b64_tr_b16 v[206:207], v12 offset:28672\n"
    "ds_read_b64_tr_b16 v[144:145], v11 offset:32768\n"
    "ds_read_b64_tr_b16 v[208:209], v12 offset:32768\n"
    "ds_read_b64_tr_b16 v[146:147], v11 offset:36864\n"
    "ds_read_b64_tr_b16 v[210:211], v12 offset:36864\n"
    "ds_read_b64_tr_b16 v[148:149], v11 offset:40960\n"
    "ds_read_b64_tr_b16 v[212:213], v12 offset:40960\n"
    "ds_read_b64_tr_b16 v[150:151], v11 offset:45056\n"
    "ds_read_b64_tr_b16 v[214:215], v12 offset:45056\n"
    "ds_read_b64_tr_b16 v[152:153], v11 offset:49152\n"
    "ds_read_b64_tr_b16 v[216:217], v12 offset:49152\n"
    "ds_read_b64_tr_b16 v[154:155], v11 offset:53248\n"
    "ds_read_b64_tr_b16 v[218:219], v12 offset:53248\n"
    "ds_read_b64_tr_b16 v[156:157], v11 offset:57344\n"
    "ds_read_b64_tr_b16 v[220:221], v12 offset:57344\n"
    "ds_read_b64_tr_b16 v[158:159], v11 offset:61440\n"
    "ds_read_b64_tr_b16 v[222:223], v12 offset:61440\n"
    "s_waitcnt lgkmcnt(0)\n"
    "s_barrier\n"
    "s_mov_b64 s[72:73], %7\n"
    "s_add_u32 m0, s49, 0\n"
    "s_add_u32 s52, s47, 0x0\n"
    "buffer_load_dwordx4 v9, s[72:75], s52 offen lds\n"
    "s_add_u32 m0, m0, 4096\n"
    "s_add_u32 s52, s47, 0x40000\n"
    "buffer_load_dwordx4 v9, s[72:75], s52 offen lds\n"
    "s_add_u32 m0, m0, 4096\n"
    "s_add_u32 s52, s47, 0x10000\n"
    "buffer_load_dwordx4 v9, s[72:75], s52 offen lds\n"
    "s_add_u32 m0, m0, 4096\n"
    "s_add_u32 s52, s47, 0x50000\n"
    "buffer_load_dwordx4 v9, s[72:75], s52 offen lds\n"
    "s_add_u32 m0, m0, 4096\n"
    "s_add_u32 s52, s47, 0x80000\n"
    "buffer_load_dwordx4 v9, s[72:75], s52 offen lds\n"
    "s_add_u32 m0, m0, 4096\n"
    "s_add_u32 s52, s47, 0xc0000\n"
    "buffer_load_dwordx4 v9, s[72:75], s52 offen lds\n"
    "s_add_u32 m0, m0, 4096\n"
    "s_add_u32 s52, s47, 0x90000\n"
    "buffer_load_dwordx4 v9, s[72:75], s52 offen lds\n"
    "s_add_u32 m0, m0, 4096\n"
    "s_add_u32 s52, s47, 0xd0000\n"
    "buffer_load_dwordx4 v9, s[72:75], s52 offen lds\n"
    "s_add_u32 m0, m0, 4096\n"
    "s_add_u32 s52, s47, 0x100000\n"
    "buffer_load_dwordx4 v9, s[72:75], s52 offen lds\n"
    "s_add_u32 m0, m0, 4096\n"
    "s_add_u32 s52, s47, 0x140000\n"
    "buffer_load_dwordx4 v9, s[72:75], s52 offen lds\n"
    "s_add_u32 m0, m0, 4096\n"
    "s_add_u32 s52, s47, 0x110000\n"
    "buffer_load_dwordx4 v9, s[72:75], s52 offen lds\n"
    "s_add_u32 m0, m0, 4096\n"
    "s_add_u32 s52, s47, 0x150000\n"
    "buffer_load_dwordx4 v9, s[72:75], s52 offen lds\n"
    "s_add_u32 m0, m0, 4096\n"
    "s_add_u32 s52, s47, 0x180000\n"
    "buffer_load_dwordx4 v9, s[72:75], s52 offen lds\n"
    "s_add_u32 m0, m0, 4096\n"
    "s_add_u32 s52, s47, 0x1c0000\n"
    "buffer_load_dwordx4 v9, s[72:75], s52 offen lds\n"
    "s_add_u32 m0, m0, 4096\n"
    "s_add_u32 s52, s47, 0x190000\n"
    "buffer_load_dwordx4 v9, s[72:75], s52 offen lds\n"
    "s_add_u32 m0, m0, 4096\n"
    "s_add_u32 s52, s47, 0x1d0000\n"
    "buffer_load_dwordx4 v9, s[72:75], s52 offen lds\n"
    "s_waitcnt vmcnt(16)\n"
    "s_barrier\n"
    "v_add_u32 v11, 0x10000, v11\n"
    "v_add_u32 v12, 0x10000, v12\n"
    "ds_read_b64_tr_b16 v[160:161], v11 offset:0\n"
    "ds_read_b64_tr_b16 v[224:225], v12 offset:0\n"
    "ds_read_b64_tr_b16 v[162:163], v11 offset:4096\n"
    "ds_read_b64_tr_b16 v[226:227], v12 offset:4096\n"
    "ds_read_b64_tr_b16 v[164:165], v11 offset:8192\n"
    "ds_read_b64_tr_b16 v[228:229], v12 offset:8192\n"
    "ds_read_b64_tr_b16 v[166:167], v11 offset:12288\n"
    "ds_read_b64_tr_b16 v[230:231], v12 offset:12288\n"
    "ds_read_b64_tr_b16 v[168:169], v11 offset:16384\n"
    "ds_read_b64_tr_b16 v[232:233], v12 offset:16384\n"
    "ds_read_b64_tr_b16 v[170:171], v11 offset:20480\n"
    "ds_read_b64_tr_b16 v[234:235], v12 offset:20480\n"
    "ds_read_b64_tr_b16 v[172:173], v11 offset:24576\n"
    "ds_read_b64_tr_b16 v[236:237], v12 offset:24576\n"
    "ds_read_b64_tr_b16 v[174:175], v11 offset:28672\n"
    "ds_read_b64_tr_b16 v[238:239], v12 offset:28672\n"
    "ds_read_b64_tr_b16 v[176:177], v11 offset:32768\n"
    "ds_read_b64_tr_b16 v[240:241], v12 offset:32768\n"
    "ds_read_b64_tr_b16 v[178:179], v11 offset:36864\n"
    "ds_read_b64_tr_b16 v[242:243], v12 offset:36864\n"
    "ds_read_b64_tr_b16 v[180:181], v11 offset:40960\n"
    "ds_read_b64_tr_b16 v[244:245], v12 offset:40960\n"
    "ds_read_b64_tr_b16 v[182:183], v11 offset:45056\n"
    "ds_read_b64_tr_b16 v[246:247], v12 offset:45056\n"
    "ds_read_b64_tr_b16 v[184:185], v11 offset:49152\n"
    "ds_read_b64_tr_b16 v[248:249], v12 offset:49152\n"
    "ds_read_b64_tr_b16 v[186:187], v11 offset:53248\n"
    "ds_read_b64_tr_b16 v[250:251], v12 offset:53248\n"
    "ds_read_b64_tr_b16 v[188:189], v11 offset:57344\n"
    "ds_read_b64_tr_b16 v[252:253], v12 offset:57344\n"
    "ds_read_b64_tr_b16 v[190:191], v11 offset:61440\n"
    "ds_read_b64_tr_b16 v[254:255], v12 offset:61440\n"
    "v_subrev_u32 v11, 0x10000, v11\n"
    "v_subrev_u32 v12, 0x10000, v12\n"
    "s_waitcnt lgkmcnt(0)\n"
    "s_barrier\n"
    "s_mov_b64 s[72:73], %7\n"
    "s_add_u32 m0, s49, 65536\n"
    "s_add_u32 s52, s47, 0x200000\n"
    "buffer_load_dwordx4 v9, s[72:75], s52 offen lds\n"
    "s_add_u32 m0, m0, 4096\n"
    "s_add_u32 s52, s47, 0x240000\n"
    "buffer_load_dwordx4 v9, s[72:75], s52 offen lds\n"
    "s_add_u32 m0, m0, 4096\n"
    "s_add_u32 s52, s47, 0x210000\n"
    "buffer_load_dwordx4 v9, s[72:75], s52 offen lds\n"
    "s_add_u32 m0, m0, 4096\n"
    "s_add_u32 s52, s47, 0x250000\n"
    "buffer_load_dwordx4 v9, s[72:75], s52 offen lds\n"
    "s_add_u32 m0, m0, 4096\n"
    "s_add_u32 s52, s47, 0x280000\n"
    "buffer_load_dwordx4 v9, s[72:75], s52 offen lds\n"
    "s_add_u32 m0, m0, 4096\n"
    "s_add_u32 s52, s47, 0x2c0000\n"
    "buffer_load_dwordx4 v9, s[72:75], s52 offen lds\n"
    "s_add_u32 m0, m0, 4096\n"
    "s_add_u32 s52, s47, 0x290000\n"
    "buffer_load_dwordx4 v9, s[72:75], s52 offen lds\n"
    "s_add_u32 m0, m0, 4096\n"
    "s_add_u32 s52, s47, 0x2d0000\n"
    "buffer_load_dwordx4 v9, s[72:75], s52 offen lds\n"
    "s_add_u32 m0, m0, 4096\n"
    "s_add_u32 s52, s47, 0x300000\n"
    "buffer_load_dwordx4 v9, s[72:75], s52 offen lds\n"
    "s_add_u32 m0, m0, 4096\n"
    "s_add_u32 s52, s47, 0x340000\n"
    "buffer_load_dwordx4 v9, s[72:75], s52 offen lds\n"
    "s_add_u32 m0, m0, 4096\n"
    "s_add_u32 s52, s47, 0x310000\n"
    "buffer_load_dwordx4 v9, s[72:75], s52 offen lds\n"
    "s_add_u32 m0, m0, 4096\n"
    "s_add_u32 s52, s47, 0x350000\n"
    "buffer_load_dwordx4 v9, s[72:75], s52 offen lds\n"
    "s_add_u32 m0, m0, 4096\n"
    "s_add_u32 s52, s47, 0x380000\n"
    "buffer_load_dwordx4 v9, s[72:75], s52 offen lds\n"
    "s_add_u32 m0, m0, 4096\n"
    "s_add_u32 s52, s47, 0x3c0000\n"
    "buffer_load_dwordx4 v9, s[72:75], s52 offen lds\n"
    "s_add_u32 m0, m0, 4096\n"
    "s_add_u32 s52, s47, 0x390000\n"
    "buffer_load_dwordx4 v9, s[72:75], s52 offen lds\n"
    "s_add_u32 m0, m0, 4096\n"
    "s_add_u32 s52, s47, 0x3d0000\n"
    "buffer_load_dwordx4 v9, s[72:75], s52 offen lds\n"
    "s_waitcnt vmcnt(16)\n"
    "s_barrier\n"
    "ds_read_b64_tr_b16 v[72:73], v11 offset:0\n"
    "ds_read_b64_tr_b16 v[80:81], v12 offset:0\n"
    "ds_read_b64_tr_b16 v[74:75], v11 offset:4096\n"
    "ds_read_b64_tr_b16 v[82:83], v12 offset:4096\n"
    "ds_read_b64_tr_b16 v[76:77], v11 offset:8192\n"
    "ds_read_b64_tr_b16 v[84:85], v12 offset:8192\n"
    "ds_read_b64_tr_b16 v[78:79], v11 offset:12288\n"
    "ds_read_b64_tr_b16 v[86:87], v12 offset:12288\n"
    "ds_read_b64_tr_b16 a[56:57], v11 offset:16384\n"
    "ds_read_b64_tr_b16 a[96:97], v12 offset:16384\n"
    "ds_read_b64_tr_b16 a[58:59], v11 offset:20480\n"
    "ds_read_b64_tr_b16 a[98:99], v12 offset:20480\n"
    "ds_read_b64_tr_b16 a[60:61], v11 offset:24576\n"
    "ds_read_b64_tr_b16 a[100:101], v12 offset:24576\n"
    "ds_read_b64_tr_b16 a[62:63], v11 offset:28672\n"
    "ds_read_b64_tr_b16 a[102:103], v12 offset:28672\n"
    "ds_read_b64_tr_b16 v[88:89], v11 offset:32768\n"
    "ds_read_b64_tr_b16 v[96:97], v12 offset:32768\n"
    "ds_read_b64_tr_b16 v[90:91], v11 offset:36864\n"
    "ds_read_b64_tr_b16 v[98:99], v12 offset:36864\n"
    "ds_read_b64_tr_b16 v[92:93], v11 offset:40960\n"
    "ds_read_b64_tr_b16 v[100:101], v12 offset:40960\n"
    "ds_read_b64_tr_b16 v[94:95], v11 offset:45056\n"
    "ds_read_b64_tr_b16 v[102:103], v12 offset:45056\n"
    "ds_read_b64_tr_b16 a[64:65], v11 offset:49152\n"
    "ds_read_b64_tr_b16 a[104:105], v12 offset:49152\n"
    "ds_read_b64_tr_b16 a[66:67], v11 offset:53248\n"
    "ds_read_b64_tr_b16 a[106:107], v12 offset:53248\n"
    "ds_read_b64_tr_b16 a[68:69], v11 offset:57344\n"
    "ds_read_b64_tr_b16 a[108:109], v12 offset:57344\n"
    "ds_read_b64_tr_b16 a[70:71], v11 offset:61440\n"
    "ds_read_b64_tr_b16 a[110:111], v12 offset:61440\n"
    "s_waitcnt lgkmcnt(0)\n"
    "s_barrier\n"
    "s_cmp_ge_u32 s60, 2\n"
    "s_cbranch_scc1 LHEAD_W23_%=\n"
    "s_mov_b64 s[72:73], %10\n"
    "s_mov_b32 m0, s50\n"
    "s_add_u32 s52, s48, 0x0\n"
    "buffer_load_dwordx4 v10, s[72:75], s52 offen lds\n"
    "s_add_u32 m0, m0, 1024\n"
    "s_add_u32 s52, s48, 0x1000\n"
    "buffer_load_dwordx4 v13, s[72:75], s52 offen lds\n"
    "s_add_u32 m0, m0, 1024\n"
    "s_add_u32 s52, s48, 0x4000\n"
    "buffer_load_dwordx4 v10, s[72:75], s52 offen lds\n"
    "s_add_u32 m0, m0, 1024\n"
    "s_add_u32 s52, s48, 0x5000\n"
    "buffer_load_dwordx4 v13, s[72:75], s52 offen lds\n"
    "s_add_u32 m0, m0, 1024\n"
    "s_add_u32 s52, s48, 0x800\n"
    "buffer_load_dwordx4 v10, s[72:75], s52 offen lds\n"
    "s_add_u32 m0, m0, 1024\n"
    "s_add_u32 s52, s48, 0x1800\n"
    "buffer_load_dwordx4 v13, s[72:75], s52 offen lds\n"
    "s_add_u32 m0, m0, 1024\n"
    "s_add_u32 s52, s48, 0x4800\n"
    "buffer_load_dwordx4 v10, s[72:75], s52 offen lds\n"
    "s_add_u32 m0, m0, 1024\n"
    "s_add_u32 s52, s48, 0x5800\n"
    "buffer_load_dwordx4 v13, s[72:75], s52 offen lds\n"
    "s_add_u32 m0, m0, 1024\n"
    "s_add_u32 s52, s48, 0x8000\n"
    "buffer_load_dwordx4 v10, s[72:75], s52 offen lds\n"
    "s_add_u32 m0, m0, 1024\n"
    "s_add_u32 s52, s48, 0x9000\n"
    "buffer_load_dwordx4 v13, s[72:75], s52 offen lds\n"
    "s_add_u32 m0, m0, 1024\n"
    "s_add_u32 s52, s48, 0xc000\n"
    "buffer_load_dwordx4 v10, s[72:75], s52 offen lds\n"
    "s_add_u32 m0, m0, 1024\n"
    "s_add_u32 s52, s48, 0xd000\n"
    "buffer_load_dwordx4 v13, s[72:75], s52 offen lds\n"
    "s_add_u32 m0, m0, 1024\n"
    "s_add_u32 s52, s48, 0x8800\n"
    "buffer_load_dwordx4 v10, s[72:75], s52 offen lds\n"
    "s_add_u32 m0, m0, 1024\n"
    "s_add_u32 s52, s48, 0x9800\n"
    "buffer_load_dwordx4 v13, s[72:75], s52 offen lds\n"
    "s_add_u32 m0, m0, 1024\n"
    "s_add_u32 s52, s48, 0xc800\n"
    "buffer_load_dwordx4 v10, s[72:75], s52 offen lds\n"
    "s_add_u32 m0, m0, 1024\n"
    "s_add_u32 s52, s48, 0xd800\n"
    "buffer_load_dwordx4 v13, s[72:75], s52 offen lds\n"
    "s_add_u32 m0, m0, 1024\n"
    "s_add_u32 s52, s48, 0x10000\n"
    "buffer_load_dwordx4 v10, s[72:75], s52 offen lds\n"
    "s_add_u32 m0, m0, 1024\n"
    "s_add_u32 s52, s48, 0x11000\n"
    "buffer_load_dwordx4 v13, s[72:75], s52 offen lds\n"
    "s_add_u32 m0, m0, 1024\n"
    "s_add_u32 s52, s48, 0x14000\n"
    "buffer_load_dwordx4 v10, s[72:75], s52 offen lds\n"
    "s_add_u32 m0, m0, 1024\n"
    "s_add_u32 s52, s48, 0x15000\n"
    "buffer_load_dwordx4 v13, s[72:75], s52 offen lds\n"
    "s_add_u32 m0, m0, 1024\n"
    "s_add_u32 s52, s48, 0x10800\n"
    "buffer_load_dwordx4 v10, s[72:75], s52 offen lds\n"
    "s_add_u32 m0, m0, 1024\n"
    "s_add_u32 s52, s48, 0x11800\n"
    "buffer_load_dwordx4 v13, s[72:75], s52 offen lds\n"
    "s_add_u32 m0, m0, 1024\n"
    "s_add_u32 s52, s48, 0x14800\n"
    "buffer_load_dwordx4 v10, s[72:75], s52 offen lds\n"
    "s_add_u32 m0, m0, 1024\n"
    "s_add_u32 s52, s48, 0x15800\n"
    "buffer_load_dwordx4 v13, s[72:75], s52 offen lds\n"
    "s_add_u32 m0, m0, 1024\n"
    "s_add_u32 s52, s48, 0x18000\n"
    "buffer_load_dwordx4 v10, s[72:75], s52 offen lds\n"
    "s_add_u32 m0, m0, 1024\n"
    "s_add_u32 s52, s48, 0x19000\n"
    "buffer_load_dwordx4 v13, s[72:75], s52 offen lds\n"
    "s_add_u32 m0, m0, 1024\n"
    "s_add_u32 s52, s48, 0x1c000\n"
    "buffer_load_dwordx4 v10, s[72:75], s52 offen lds\n"
    "s_add_u32 m0, m0, 1024\n"
    "s_add_u32 s52, s48, 0x1d000\n"
    "buffer_load_dwordx4 v13, s[72:75], s52 offen lds\n"
    "s_add_u32 m0, m0, 1024\n"
    "s_add_u32 s52, s48, 0x18800\n"
    "buffer_load_dwordx4 v10, s[72:75], s52 offen lds\n"
    "s_add_u32 m0, m0, 1024\n"
    "s_add_u32 s52, s48, 0x19800\n"
    "buffer_load_dwordx4 v13, s[72:75], s52 offen lds\n"
    "s_add_u32 m0, m0, 1024\n"
    "s_add_u32 s52, s48, 0x1c800\n"
    "buffer_load_dwordx4 v10, s[72:75], s52 offen lds\n"
    "s_add_u32 m0, m0, 1024\n"
    "s_add_u32 s52, s48, 0x1d800\n"
    "buffer_load_dwordx4 v13, s[72:75], s52 offen lds\n"
    "s_waitcnt vmcnt(32)\n"
    "s_barrier\n"
    "v_add_u32 v11, 0x10000, v11\n"
    "v_add_u32 v12, 0x10000, v12\n"
    "ds_read_b64_tr_b16 v[104:105], v11 offset:0\n"
    "ds_read_b64_tr_b16 v[112:113], v12 offset:0\n"
    "ds_read_b64_tr_b16 v[106:107], v11 offset:4096\n"
    "ds_read_b64_tr_b16 v[114:115], v12 offset:4096\n"
    "ds_read_b64_tr_b16 v[108:109], v11 offset:8192\n"
    "ds_read_b64_tr_b16 v[116:117], v12 offset:8192\n"
    "ds_read_b64_tr_b16 v[110:111], v11 offset:12288\n"
    "ds_read_b64_tr_b16 v[118:119], v12 offset:12288\n"
    "ds_read_b64_tr_b16 a[72:73], v11 offset:16384\n"
    "ds_read_b64_tr_b16 a[112:113], v12 offset:16384\n"
    "ds_read_b64_tr_b16 a[74:75], v11 offset:20480\n"
    "ds_read_b64_tr_b16 a[114:115], v12 offset:20480\n"
    "ds_read_b64_tr_b16 a[76:77], v11 offset:24576\n"
    "ds_read_b64_tr_b16 a[116:117], v12 offset:24576\n"
    "ds_read_b64_tr_b16 a[78:79], v11 offset:28672\n"
    "ds_read_b64_tr_b16 a[118:119], v12 offset:28672\n"
    "ds_read_b64_tr_b16 a[48:49], v11 offset:32768\n"
    "ds_read_b64_tr_b16 a[88:89], v12 offset:32768\n"
    "ds_read_b64_tr_b16 a[50:51], v11 offset:36864\n"
    "ds_read_b64_tr_b16 a[90:91], v12 offset:36864\n"
    "ds_read_b64_tr_b16 a[52:53], v11 offset:40960\n"
    "ds_read_b64_tr_b16 a[92:93], v12 offset:40960\n"
    "ds_read_b64_tr_b16 a[54:55], v11 offset:45056\n"
    "ds_read_b64_tr_b16 a[94:95], v12 offset:45056\n"
    "ds_read_b64_tr_b16 a[80:81], v11 offset:49152\n"
    "ds_read_b64_tr_b16 a[120:121], v12 offset:49152\n"
    "ds_read_b64_tr_b16 a[82:83], v11 offset:53248\n"
    "ds_read_b64_tr_b16 a[122:123], v12 offset:53248\n"
    "ds_read_b64_tr_b16 a[84:85], v11 offset:57344\n"
    "ds_read_b64_tr_b16 a[124:125], v12 offset:57344\n"
    "ds_read_b64_tr_b16 a[86:87], v11 offset:61440\n"
    "ds_read_b64_tr_b16 a[126:127], v12 offset:61440\n"
    "v_subrev_u32 v11, 0x10000, v11\n"
    "v_subrev_u32 v12, 0x10000, v12\n"
    "s_waitcnt lgkmcnt(0)\n"
    "s_barrier\n"
    "s_branch LHEAD_WJ_%=\n"
    "LHEAD_W23_%=:\n"
    "s_waitcnt vmcnt(0)\n"
    "s_barrier\n"
    "v_add_u32 v11, 0x10000, v11\n"
    "v_add_u32 v12, 0x10000, v12\n"
    "ds_read_b64_tr_b16 v[104:105], v11 offset:0\n"
    "ds_read_b64_tr_b16 v[112:113], v12 offset:0\n"
    "ds_read_b64_tr_b16 v[106:107], v11 offset:4096\n"
    "ds_read_b64_tr_b16 v[114:115], v12 offset:4096\n"
    "ds_read_b64_tr_b16 v[108:109], v11 offset:8192\n"
    "ds_read_b64_tr_b16 v[116:117], v12 offset:8192\n"
    "ds_read_b64_tr_b16 v[110:111], v11 offset:12288\n"
    "ds_read_b64_tr_b16 v[118:119], v12 offset:12288\n"
    "ds_read_b64_tr_b16 a[72:73], v11 offset:16384\n"
    "ds_read_b64_tr_b16 a[112:113], v12 offset:16384\n"
    "ds_read_b64_tr_b16 a[74:75], v11 offset:20480\n"
    "ds_read_b64_tr_b16 a[114:115], v12 offset:20480\n"
    "ds_read_b64_tr_b16 a[76:77], v11 offset:24576\n"
    "ds_read_b64_tr_b16 a[116:117], v12 offset:24576\n"
    "ds_read_b64_tr_b16 a[78:79], v11 offset:28672\n"
    "ds_read_b64_tr_b16 a[118:119], v12 offset:28672\n"
    "ds_read_b64_tr_b16 a[48:49], v11 offset:32768\n"
    "ds_read_b64_tr_b16 a[88:89], v12 offset:32768\n"
    "ds_read_b64_tr_b16 a[50:51], v11 offset:36864\n"
    "ds_read_b64_tr_b16 a[90:91], v12 offset:36864\n"
    "ds_read_b64_tr_b16 a[52:53], v11 offset:40960\n"
    "ds_read_b64_tr_b16 a[92:93], v12 offset:40960\n"
    "ds_read_b64_tr_b16 a[54:55], v11 offset:45056\n"
    "ds_read_b64_tr_b16 a[94:95], v12 offset:45056\n"
    "ds_read_b64_tr_b16 a[80:81], v11 offset:49152\n"
    "ds_read_b64_tr_b16 a[120:121], v12 offset:49152\n"
    "ds_read_b64_tr_b16 a[82:83], v11 offset:53248\n"
    "ds_read_b64_tr_b16 a[122:123], v12 offset:53248\n"
    "ds_read_b64_tr_b16 a[84:85], v11 offset:57344\n"
    "ds_read_b64_tr_b16 a[124:125], v12 offset:57344\n"
    "ds_read_b64_tr_b16 a[86:87], v11 offset:61440\n"
    "ds_read_b64_tr_b16 a[126:127], v12 offset:61440\n"
    "v_subrev_u32 v11, 0x10000, v11\n"
    "v_subrev_u32 v12, 0x10000, v12\n"
    "s_waitcnt lgkmcnt(0)\n"
    "s_barrier\n"
    "s_mov_b64 s[72:73], %10\n"
    "s_mov_b32 m0, s50\n"
    "s_add_u32 s52, s48, 0x0\n"
    "buffer_load_dwordx4 v10, s[72:75], s52 offen lds\n"
    "s_add_u32 m0, m0, 1024\n"
    "s_add_u32 s52, s48, 0x1000\n"
    "buffer_load_dwordx4 v13, s[72:75], s52 offen lds\n"
    "s_add_u32 m0, m0, 1024\n"
    "s_add_u32 s52, s48, 0x4000\n"
    "buffer_load_dwordx4 v10, s[72:75], s52 offen lds\n"
    "s_add_u32 m0, m0, 1024\n"
    "s_add_u32 s52, s48, 0x5000\n"
    "buffer_load_dwordx4 v13, s[72:75], s52 offen lds\n"
    "s_add_u32 m0, m0, 1024\n"
    "s_add_u32 s52, s48, 0x800\n"
    "buffer_load_dwordx4 v10, s[72:75], s52 offen lds\n"
    "s_add_u32 m0, m0, 1024\n"
    "s_add_u32 s52, s48, 0x1800\n"
    "buffer_load_dwordx4 v13, s[72:75], s52 offen lds\n"
    "s_add_u32 m0, m0, 1024\n"
    "s_add_u32 s52, s48, 0x4800\n"
    "buffer_load_dwordx4 v10, s[72:75], s52 offen lds\n"
    "s_add_u32 m0, m0, 1024\n"
    "s_add_u32 s52, s48, 0x5800\n"
    "buffer_load_dwordx4 v13, s[72:75], s52 offen lds\n"
    "s_add_u32 m0, m0, 1024\n"
    "s_add_u32 s52, s48, 0x8000\n"
    "buffer_load_dwordx4 v10, s[72:75], s52 offen lds\n"
    "s_add_u32 m0, m0, 1024\n"
    "s_add_u32 s52, s48, 0x9000\n"
    "buffer_load_dwordx4 v13, s[72:75], s52 offen lds\n"
    "s_add_u32 m0, m0, 1024\n"
    "s_add_u32 s52, s48, 0xc000\n"
    "buffer_load_dwordx4 v10, s[72:75], s52 offen lds\n"
    "s_add_u32 m0, m0, 1024\n"
    "s_add_u32 s52, s48, 0xd000\n"
    "buffer_load_dwordx4 v13, s[72:75], s52 offen lds\n"
    "s_add_u32 m0, m0, 1024\n"
    "s_add_u32 s52, s48, 0x8800\n"
    "buffer_load_dwordx4 v10, s[72:75], s52 offen lds\n"
    "s_add_u32 m0, m0, 1024\n"
    "s_add_u32 s52, s48, 0x9800\n"
    "buffer_load_dwordx4 v13, s[72:75], s52 offen lds\n"
    "s_add_u32 m0, m0, 1024\n"
    "s_add_u32 s52, s48, 0xc800\n"
    "buffer_load_dwordx4 v10, s[72:75], s52 offen lds\n"
    "s_add_u32 m0, m0, 1024\n"
    "s_add_u32 s52, s48, 0xd800\n"
    "buffer_load_dwordx4 v13, s[72:75], s52 offen lds\n"
    "s_add_u32 m0, m0, 1024\n"
    "s_add_u32 s52, s48, 0x10000\n"
    "buffer_load_dwordx4 v10, s[72:75], s52 offen lds\n"
    "s_add_u32 m0, m0, 1024\n"
    "s_add_u32 s52, s48, 0x11000\n"
    "buffer_load_dwordx4 v13, s[72:75], s52 offen lds\n"
    "s_add_u32 m0, m0, 1024\n"
    "s_add_u32 s52, s48, 0x14000\n"
    "buffer_load_dwordx4 v10, s[72:75], s52 offen lds\n"
    "s_add_u32 m0, m0, 1024\n"
    "s_add_u32 s52, s48, 0x15000\n"
    "buffer_load_dwordx4 v13, s[72:75], s52 offen lds\n"
    "s_add_u32 m0, m0, 1024\n"
    "s_add_u32 s52, s48, 0x10800\n"
    "buffer_load_dwordx4 v10, s[72:75], s52 offen lds\n"
    "s_add_u32 m0, m0, 1024\n"
    "s_add_u32 s52, s48, 0x11800\n"
    "buffer_load_dwordx4 v13, s[72:75], s52 offen lds\n"
    "s_add_u32 m0, m0, 1024\n"
    "s_add_u32 s52, s48, 0x14800\n"
    "buffer_load_dwordx4 v10, s[72:75], s52 offen lds\n"
    "s_add_u32 m0, m0, 1024\n"
    "s_add_u32 s52, s48, 0x15800\n"
    "buffer_load_dwordx4 v13, s[72:75], s52 offen lds\n"
    "s_add_u32 m0, m0, 1024\n"
    "s_add_u32 s52, s48, 0x18000\n"
    "buffer_load_dwordx4 v10, s[72:75], s52 offen lds\n"
    "s_add_u32 m0, m0, 1024\n"
    "s_add_u32 s52, s48, 0x19000\n"
    "buffer_load_dwordx4 v13, s[72:75], s52 offen lds\n"
    "s_add_u32 m0, m0, 1024\n"
    "s_add_u32 s52, s48, 0x1c000\n"
    "buffer_load_dwordx4 v10, s[72:75], s52 offen lds\n"
    "s_add_u32 m0, m0, 1024\n"
    "s_add_u32 s52, s48, 0x1d000\n"
    "buffer_load_dwordx4 v13, s[72:75], s52 offen lds\n"
    "s_add_u32 m0, m0, 1024\n"
    "s_add_u32 s52, s48, 0x18800\n"
    "buffer_load_dwordx4 v10, s[72:75], s52 offen lds\n"
    "s_add_u32 m0, m0, 1024\n"
    "s_add_u32 s52, s48, 0x19800\n"
    "buffer_load_dwordx4 v13, s[72:75], s52 offen lds\n"
    "s_add_u32 m0, m0, 1024\n"
    "s_add_u32 s52, s48, 0x1c800\n"
    "buffer_load_dwordx4 v10, s[72:75], s52 offen lds\n"
    "s_add_u32 m0, m0, 1024\n"
    "s_add_u32 s52, s48, 0x1d800\n"
    "buffer_load_dwordx4 v13, s[72:75], s52 offen lds\n"
    "LHEAD_WJ_%=:\n"
    "s_waitcnt vmcnt(16)\n"
    "v_xor_b32 v15, 0, v14\n"
    "ds_read_b64_tr_b16 a[0:1], v15 offset:0\n"
    "ds_read_b64_tr_b16 a[2:3], v15 offset:4096\n"
    "ds_read_b64_tr_b16 a[4:5], v15 offset:8192\n"
    "ds_read_b64_tr_b16 a[6:7], v15 offset:12288\n"
    "v_xor_b32 v15, 32, v14\n"
    "ds_read_b64_tr_b16 a[16:17], v15 offset:0\n"
    "ds_read_b64_tr_b16 a[18:19], v15 offset:4096\n"
    "ds_read_b64_tr_b16 a[20:21], v15 offset:8192\n"
    "ds_read_b64_tr_b16 a[22:23], v15 offset:12288\n"
    "v_xor_b32 v15, 64, v14\n"
    "ds_read_b64_tr_b16 a[32:33], v15 offset:0\n"
    "ds_read_b64_tr_b16 a[34:35], v15 offset:4096\n"
    "ds_read_b64_tr_b16 a[36:37], v15 offset:8192\n"
    "ds_read_b64_tr_b16 a[38:39], v15 offset:12288\n"
    "v_xor_b32 v15, 96, v14\n"
    "ds_read_b64_tr_b16 v[16:17], v15 offset:0\n"
    "ds_read_b64_tr_b16 v[18:19], v15 offset:4096\n"
    "ds_read_b64_tr_b16 v[20:21], v15 offset:8192\n"
    "ds_read_b64_tr_b16 v[22:23], v15 offset:12288\n"
    "v_xor_b32 v15, 128, v14\n"
    "ds_read_b64_tr_b16 v[24:25], v15 offset:0\n"
    "ds_read_b64_tr_b16 v[26:27], v15 offset:4096\n"
    "ds_read_b64_tr_b16 v[28:29], v15 offset:8192\n"
    "ds_read_b64_tr_b16 v[30:31], v15 offset:12288\n"
    "v_xor_b32 v15, 160, v14\n"
    "ds_read_b64_tr_b16 v[32:33], v15 offset:0\n"
    "ds_read_b64_tr_b16 v[34:35], v15 offset:4096\n"
    "ds_read_b64_tr_b16 v[36:37], v15 offset:8192\n"
    "ds_read_b64_tr_b16 v[38:39], v15 offset:12288\n"
    "v_xor_b32 v15, 192, v14\n"
    "ds_read_b64_tr_b16 v[40:41], v15 offset:0\n"
    "ds_read_b64_tr_b16 v[42:43], v15 offset:4096\n"
    "ds_read_b64_tr_b16 v[44:45], v15 offset:8192\n"
    "ds_read_b64_tr_b16 v[46:47], v15 offset:12288\n"
    "v_xor_b32 v15, 224, v14\n"
    "ds_read_b64_tr_b16 v[120:121], v15 offset:0\n"
    "ds_read_b64_tr_b16 v[122:123], v15 offset:4096\n"
    "ds_read_b64_tr_b16 v[124:125], v15 offset:8192\n"
    "ds_read_b64_tr_b16 v[126:127], v15 offset:12288\n"
    "s_waitcnt lgkmcnt(0)\n"
    "ds_write_b128 v7, v[16:19] offset:0\n"
    "ds_write_b128 v7, v[20:23] offset:1024\n"
    "ds_write_b128 v7, v[24:27] offset:4096\n"
    "ds_write_b128 v7, v[28:31] offset:5120\n"
    "ds_write_b128 v7, v[32:35] offset:8192\n"
    "ds_write_b128 v7, v[36:39] offset:9216\n"
    "ds_write_b128 v7, v[40:43] offset:12288\n"
    "ds_write_b128 v7, v[44:47] offset:13312\n"
    "s_waitcnt vmcnt(0) lgkmcnt(0)\n"
    "v_xor_b32 v15, 0, v14\n"
    "ds_read_b64_tr_b16 a[8:9], v15 offset:16384\n"
    "ds_read_b64_tr_b16 a[10:11], v15 offset:20480\n"
    "ds_read_b64_tr_b16 a[12:13], v15 offset:24576\n"
    "ds_read_b64_tr_b16 a[14:15], v15 offset:28672\n"
    "v_xor_b32 v15, 32, v14\n"
    "ds_read_b64_tr_b16 a[24:25], v15 offset:16384\n"
    "ds_read_b64_tr_b16 a[26:27], v15 offset:20480\n"
    "ds_read_b64_tr_b16 a[28:29], v15 offset:24576\n"
    "ds_read_b64_tr_b16 a[30:31], v15 offset:28672\n"
    "v_xor_b32 v15, 64, v14\n"
    "ds_read_b64_tr_b16 a[40:41], v15 offset:16384\n"
    "ds_read_b64_tr_b16 a[42:43], v15 offset:20480\n"
    "ds_read_b64_tr_b16 a[44:45], v15 offset:24576\n"
    "ds_read_b64_tr_b16 a[46:47], v15 offset:28672\n"
    "v_xor_b32 v15, 96, v14\n"
    "ds_read_b64_tr_b16 v[16:17], v15 offset:16384\n"
    "ds_read_b64_tr_b16 v[18:19], v15 offset:20480\n"
    "ds_read_b64_tr_b16 v[20:21], v15 offset:24576\n"
    "ds_read_b64_tr_b16 v[22:23], v15 offset:28672\n"
    "v_xor_b32 v15, 128, v14\n"
    "ds_read_b64_tr_b16 v[24:25], v15 offset:16384\n"
    "ds_read_b64_tr_b16 v[26:27], v15 offset:20480\n"
    "ds_read_b64_tr_b16 v[28:29], v15 offset:24576\n"
    "ds_read_b64_tr_b16 v[30:31], v15 offset:28672\n"
    "v_xor_b32 v15, 160, v14\n"
    "ds_read_b64_tr_b16 v[32:33], v15 offset:16384\n"
    "ds_read_b64_tr_b16 v[34:35], v15 offset:20480\n"
    "ds_read_b64_tr_b16 v[36:37], v15 offset:24576\n"
    "ds_read_b64_tr_b16 v[38:39], v15 offset:28672\n"
    "v_xor_b32 v15, 192, v14\n"
    "ds_read_b64_tr_b16 v[40:41], v15 offset:16384\n"
    "ds_read_b64_tr_b16 v[42:43], v15 offset:20480\n"
    "ds_read_b64_tr_b16 v[44:45], v15 offset:24576\n"
    "ds_read_b64_tr_b16 v[46:47], v15 offset:28672\n"
    "v_xor_b32 v15, 224, v14\n"
    "ds_read_b64_tr_b16 v[48:49], v15 offset:16384\n"
    "ds_read_b64_tr_b16 v[50:51], v15 offset:20480\n"
    "ds_read_b64_tr_b16 v[52:53], v15 offset:24576\n"
    "ds_read_b64_tr_b16 v[54:55], v15 offset:28672\n"
    "s_waitcnt lgkmcnt(0)\n"
    "ds_write_b128 v7, v[120:123] offset:16384\n"
    "ds_write_b128 v7, v[124:127] offset:17408\n"
    "ds_write_b128 v7, v[16:19] offset:2048\n"
    "ds_write_b128 v7, v[20:23] offset:3072\n"
    "ds_write_b128 v7, v[24:27] offset:6144\n"
    "ds_write_b128 v7, v[28:31] offset:7168\n"
    "ds_write_b128 v7, v[32:35] offset:10240\n"
    "ds_write_b128 v7, v[36:39] offset:11264\n"
    "ds_write_b128 v7, v[40:43] offset:14336\n"
    "ds_write_b128 v7, v[44:47] offset:15360\n"
    "ds_write_b128 v7, v[48:51] offset:18432\n"
    "ds_write_b128 v7, v[52:55] offset:19456\n"
    "ds_write_b128 v7, v[72:75] offset:20480\n"
    "ds_write_b128 v7, v[76:79] offset:21504\n"
    "ds_write_b128 v7, v[80:83] offset:22528\n"
    "ds_write_b128 v7, v[84:87] offset:23552\n"
    "ds_write_b128 v7, v[88:91] offset:24576\n"
    "ds_write_b128 v7, v[92:95] offset:25600\n"
    "ds_write_b128 v7, v[96:99] offset:26624\n"
    "ds_write_b128 v7, v[100:103] offset:27648\n"
    "ds_write_b128 v7, v[104:107] offset:28672\n"
    "ds_write_b128 v7, v[108:111] offset:29696\n"
    "ds_write_b128 v7, v[112:115] offset:30720\n"
    "ds_write_b128 v7, v[116:119] offset:31744\n"
    "v_mov_b32 v12, 0x3f803f80\n"
    "v_mov_b32 v13, 0x3f803f80\n"
    "v_mov_b32 v14, 0x3f803f80\n"
    "v_mov_b32 v15, 0x3f803f80\n"
    "s_cmp_lg_u32 s60, 0\n"
    "s_cbranch_scc1 LHEAD_CI_%=\n"
    "v_mov_b32 v64, s98\n"
    "v_mov_b32 v65, s99\n"
    "v_mov_b32 v66, s98\n"
    "v_mov_b32 v67, s99\n"
    "s_lshl_b32 s52, %12, 7\n"
    "s_add_u32 s52, s52, 98304\n"
    "s_mov_b64 exec, 0xff\n"
    "buffer_store_dwordx4 v[64:67], v0, s[40:43], s52 offen sc1\n"
    "s_mov_b64 exec, -1\n"
    "LHEAD_CI_%=:\n"
    "s_waitcnt lgkmcnt(0)\n"
    "s_mov_b32 s52, 8192\n"
    "s_mov_b32 s58, 0\n"
    "LHEAD_P1_%=:\n"
    "buffer_load_dwordx2 v[16:17], off, s[40:43], s52 sc1\n"
    "s_add_u32 s58, s58, 1\n"
    "s_cmp_gt_u32 s58, 0x40000\n"
    "s_cbranch_scc1 LHEAD_FAIL_%=\n"
    "s_waitcnt vmcnt(0)\n"
    "v_xor_b32 v18, s98, v16\n"
    "v_xor_b32 v19, s99, v17\n"
    "v_and_b32 v19, 0x7fffffff, v19\n"
    "v_or_b32 v18, v18, v19\n"
    "v_cmp_ne_u32 vcc, 0, v18\n"
    "s_nop 1\n"
    "s_cbranch_vccnz LHEAD_P1_%=\n"
    "v_readfirstlane_b32 s52, v17\n"
    "s_nop 1\n"
    "s_xor_b32 s52, s52, s99\n"
    "s_lshr_b32 s52, s52, 31\n"
    "s_lshl_b32 s52, s52, 13\n"
    "s_add_u32 s71, s71, s52\n"
    "s_lshl_b32 s70, s70, 14\n"
    "s_add_u32 s70, s70, s62\n"
    "s_add_u32 s70, s70, s71\n"
    "s_add_u32 s70, s70, 0x180000\n"
    "v_lshlrev_b32 v20, 3, v0\n"
    "s_mov_b32 s52, 65536\n"
    "s_mov_b32 s58, 0\n"
    "LHEAD_P2_%=:\n"
    "buffer_load_dwordx2 v[16:17], v20, s[40:43], s52 offen sc1\n"
    "s_add_u32 s58, s58, 1\n"
    "s_cmp_gt_u32 s58, 0x40000\n"
    "s_cbranch_scc1 LHEAD_FAIL_%=\n"
    "s_waitcnt vmcnt(0)\n"
    "v_xor_b32 v18, s98, v16\n"
    "v_xor_b32 v19, s99, v17\n"
    "v_or_b32 v18, v18, v19\n"
    "v_cmp_ne_u32 vcc, 0, v18\n"
    "s_nop 1\n"
    "s_cbranch_vccnz LHEAD_P2_%=\n"
    "s_and_b32 s52, s61, 15\n"
    "s_lshl_b32 s52, s52, 9\n"
    "s_lshr_b32 s57, s61, 4\n"
    "s_lshl_b32 s57, s57, 14\n"
    "s_add_u32 s52, s52, s57\n"
    "s_lshl_b32 s57, %5, 1\n"
    "s_add_u32 s52, s52, s57\n"
    "s_sub_u32 s52, s52, s71\n"
    "s_add_u32 s90, s52, 73728\n"
    "v_and_b32 v20, 0x30, v0\n"
    "v_lshlrev_b32 v20, 3, v20\n"
    "s_and_b32 s52, 0, 1\n"
    "s_lshl_b32 s52, s52, 15\n"
    "s_add_u32 s52, s52, s90\n"
    "s_add_u32 s57, s98, 0\n"
    "s_add_u32 s57, s57, 1\n"
    "s_mov_b32 s58, 0\n"
    "LHEAD_P3_%=:\n"
    "buffer_load_dwordx2 v[16:17], v20, s[40:43], s52 offen sc1\n"
    "s_add_u32 s58, s58, 1\n"
    "s_cmp_gt_u32 s58, 0x40000\n"
    "s_cbranch_scc1 LHEAD_FAIL_%=\n"
    "s_waitcnt vmcnt(0)\n"
    "v_subrev_u32 v18, s57, v16\n"
    "v_lshrrev_b32 v18, 6, v18\n"
    "v_xor_b32 v19, s99, v17\n"
    "v_or_b32 v18, v18, v19\n"
    "v_cmp_ne_u32 vcc, 0, v18\n"
    "s_nop 1\n"
    "s_cbranch_vccnz LHEAD_P3_%=\n"
    "s_lshl_b32 s56, s61, 21\n"
    "s_add_u32 s56, s56, s62\n"
    "s_add_u32 s56, s56, %5\n"
    "s_sub_u32 s56, s56, 0x4000000\n"
    "s_add_u32 m0, s62, 8192\n"
    "s_nop 0\n"
    "buffer_load_dwordx4 v0, s[40:43], s56 offen sc1 lds\n"
    "buffer_load_dwordx4 v0, s[40:43], s56 offen offset:1024 sc1 lds\n"
    "s_add_u32 s52, s56, 0x80000\n"
    "buffer_load_dwordx4 v[120:123], v0, s[40:43], s52 offen sc1\n"
    "buffer_load_dwordx4 v[124:127], v0, s[40:43], s52 offen offset:1024 sc1\n"
    "v_mov_b64 v[48:49], 0\n"
    "v_mov_b64 v[50:51], 0\n"
    "v_mov_b64 v[52:53], 0\n"
    "v_mov_b64 v[54:55], 0\n"
    "v_mov_b64 v[56:57], 0\n"
    "v_mov_b64 v[58:59], 0\n"
    "v_mov_b64 v[60:61], 0\n"
    "v_mov_b64 v[62:63], 0\n"
    "v_mov_b64 v[64:65], 0\n"
    "v_mov_b64 v[66:67], 0\n"
    "v_mov_b64 v[68:69], 0\n"
    "v_mov_b64 v[70:71], 0\n"
    "s_add_u32 s53, s71, 0x100000\n"
    "s_mov_b32 s54, s71\n"
    "s_mov_b32 s55, s70\n"
    "s_waitcnt lgkmcnt(0)\n"
    "s_barrier\n"
    "s_memrealtime s[48:49]\n"
    "s_memtime s[86:87]\n"
    "s_waitcnt lgkmcnt(0)\n"
    "s_sub_u32 s84, s48, s84\n"
    "s_sub_u32 s83, s86, s83\n"
    "v_cvt_f32_u32 v16, s84\n"
    "v_cvt_f32_u32 v17, s83\n"
    "v_rcp_f32 v16, v16\n"
    "v_mul_f32 v17, 0x43800000, v17\n"
    "v_mul_f32 v16, v16, v17\n"
    "v_cvt_u32_f32 v16, v16\n"
    "s_nop 0\n"
    "v_readfirstlane_b32 s82, v16\n"
    "s_nop 1\n"
    "s_max_u32 s82, s82, 4000\n"
    "s_min_u32 s82, s82, 6600\n"
    "s_mov_b32 s84, s48\n"
    "s_mov_b32 s83, s86\n"
    "s_mov_b32 s85, s83\n"
    "s_waitcnt vmcnt(0)\n"
    "s_nop 3\n"
    "LHEAD_LOOP_%=:\n"
    "s_waitcnt vmcnt(1) lgkmcnt(0)\n"
    "s_barrier\n"
    "ds_read_b128 v[16:19], v0 offset:0\n"
    "ds_read_b128 v[24:27], v0 offset:2048\n"
    "ds_read_b128 v[32:35], v0 offset:4096\n"
    "ds_read_b128 v[40:43], v0 offset:6144\n"
    "s_waitcnt lgkmcnt(3)\n"
    "v_smfmac_f32_16x16x64_bf16 v[48:51], v[16:19], v[72:79], v1\n"
    "v_smfmac_f32_16x16x64_bf16 v[52:55], v[16:19], v[80:87], v1\n"
    "s_waitcnt lgkmcnt(2)\n"
    "v_smfmac_f32_16x16x64_bf16 v[48:51], v[24:27], v[88:95], v1\n"
    "v_smfmac_f32_16x16x64_bf16 v[52:55], v[24:27], v[96:103], v1\n"
    "s_waitcnt lgkmcnt(0)\n"
    "v_smfmac_f32_16x16x64_bf16 v[48:51], v[32:35], v[104:111], v1\n"
    "v_smfmac_f32_16x16x64_bf16 v[52:55], v[32:35], v[112:119], v1\n"
    "v_smfmac_f32_16x16x64_bf16 v[48:51], v[40:43], a[48:55], v1\n"
    "v_smfmac_f32_16x16x64_bf16 v[52:55], v[40:43], a[88:95], v1\n"
    "s_waitcnt vmcnt(0)\n"
    "s_barrier\n"
    "v_smfmac_f32_16x16x64_bf16 v[56:59], v[16:19], a[128:135], v1\n"
    "ds_read_b128 v[20:23], v0 offset:1024\n"
    "v_max_u32 v9, v16, v18\n"
    "v_smfmac_f32_16x16x64_bf16 v[60:63], v[16:19], a[192:199], v1\n"
    "ds_read_b128 v[28:31], v0 offset:3072\n"
    "v_max3_u32 v9, v9, v24, v26\n"
    "v_smfmac_f32_16x16x64_bf16 v[56:59], v[24:27], a[144:151], v1\n"
    "ds_read_b128 v[36:39], v0 offset:5120\n"
    "v_max3_u32 v9, v9, v32, v34\n"
    "v_smfmac_f32_16x16x64_bf16 v[60:63], v[24:27], a[208:215], v1\n"
    "ds_read_b128 v[44:47], v0 offset:7168\n"
    "s_waitcnt lgkmcnt(3)\n"
    "v_smfmac_f32_16x16x64_bf16 v[48:51], v[20:23], a[56:63], v1\n"
    "v_max3_u32 v9, v9, v40, v42\n"
    "v_smfmac_f32_16x16x64_bf16 v[52:55], v[20:23], a[96:103], v1\n"
    "s_waitcnt lgkmcnt(2)\n"
    "v_max3_u32 v9, v9, v20, v22\n"
    "v_smfmac_f32_16x16x64_bf16 v[48:51], v[28:31], a[64:71], v1\n"
    "v_max3_u32 v9, v9, v28, v30\n"
    "v_smfmac_f32_16x16x64_bf16 v[52:55], v[28:31], a[104:111], v1\n"
    "s_waitcnt lgkmcnt(0)\n"
    "v_max3_u32 v9, v9, v36, v38\n"
    "v_smfmac_f32_16x16x64_bf16 v[48:51], v[36:39], a[72:79], v1\n"
    "ds_read_b128 v[88:91], v0 offset:8192\n"
    "v_max3_u32 v9, v9, v44, v46\n"
    "v_smfmac_f32_16x16x64_bf16 v[52:55], v[36:39], a[112:119], v1\n"
    "ds_read_b128 v[104:107], v0 offset:12288\n"
    "v_cmp_eq_u32 vcc, -1, v9\n"
    "v_smfmac_f32_16x16x64_bf16 v[48:51], v[44:47], a[80:87], v1\n"
    "ds_read_b128 v[92:95], v0 offset:9216\n"
    "v_smfmac_f32_16x16x64_bf16 v[52:55], v[44:47], a[120:127], v1\n"
    "s_cbranch_vccnz LHEAD_RETRY_%=\n"
    "ds_read_b128 v[108:111], v0 offset:13312\n"
    "v_smfmac_f32_16x16x64_bf16 v[56:59], v[32:35], a[160:167], v1\n"
    "ds_read_b128 v[96:99], v0 offset:10240\n"
    "s_nop 0\n"
    "v_smfmac_f32_16x16x64_bf16 v[60:63], v[32:35], a[224:231], v1\n"
    "ds_read_b128 v[112:115], v0 offset:14336\n"
    "v_add_f32 v48, v48, v50\n"
    "v_smfmac_f32_16x16x64_bf16 v[56:59], v[40:43], a[176:183], v1\n"
    "v_add_f32 v49, v49, v51\n"
    "v_add_f32 v52, v52, v54\n"
    "v_smfmac_f32_16x16x64_bf16 v[60:63], v[40:43], a[240:247], v1\n"
    "v_add_f32 v53, v53, v55\n"
    "v_cvt_pk_bf16_f32 v48, v48, v49\n"
    "v_smfmac_f32_16x16x64_bf16 v[56:59], v[20:23], a[136:143], v1\n"
    "ds_read_b128 v[100:103], v0 offset:11264\n"
    "v_cvt_pk_bf16_f32 v52, v52, v53\n"
    "v_smfmac_f32_16x16x64_bf16 v[60:63], v[20:23], a[200:207], v1\n"
    "ds_write2st64_b32 v4, v48, v52 offset1:1\n"
    "v_smfmac_f32_16x16x64_bf16 v[56:59], v[28:31], a[152:159], v1\n"
    "ds_read_b128 v[116:119], v0 offset:15360\n"
    "v_smfmac_f32_16x16x64_bf16 v[60:63], v[28:31], a[216:223], v1\n"
    "v_smfmac_f32_16x16x64_bf16 v[56:59], v[36:39], a[168:175], v1\n"
    "v_smfmac_f32_16x16x64_bf16 v[60:63], v[36:39], a[232:239], v1\n"
    "v_smfmac_f32_16x16x64_bf16 v[56:59], v[44:47], a[184:191], v1\n"
    "s_waitcnt lgkmcnt(1)\n"
    "s_barrier\n"
    "v_smfmac_f32_16x16x64_bf16 v[60:63], v[44:47], a[248:255], v1\n"
    "ds_read_b64_tr_b16 v[72:73], v2 offset:0\n"
    "ds_read_b64_tr_b16 v[74:75], v2 offset:256\n"
    "v_smfmac_f32_16x16x64_bf16 v[64:67], v[16:19], v[128:135], v1\n"
    "ds_read_b64_tr_b16 v[76:77], v2 offset:512\n"
    "ds_read_b64_tr_b16 v[78:79], v2 offset:768\n"
    "v_smfmac_f32_16x16x64_bf16 v[68:71], v[16:19], v[192:199], v1\n"
    "ds_read_b64_tr_b16 v[80:81], v2 offset:1024\n"
    "ds_read_b64_tr_b16 v[82:83], v2 offset:1280\n"
    "v_smfmac_f32_16x16x64_bf16 v[64:67], v[20:23], v[136:143], v1\n"
    "ds_read_b64_tr_b16 v[84:85], v2 offset:1536\n"
    "v_add_f32 v56, v56, v58\n"
    "v_smfmac_f32_16x16x64_bf16 v[68:71], v[20:23], v[200:207], v1\n"
    "ds_read_b64_tr_b16 v[86:87], v2 offset:1792\n"
    "s_waitcnt lgkmcnt(4)\n"
    "v_add_f32 v57, v57, v59\n"
    "v_mfma_f32_16x16x32_bf16 v[48:51], v[88:91], v[72:75], 0\n"
    "v_add_f32 v60, v60, v62\n"
    "v_add_f32 v61, v61, v63\n"
    "v_mfma_f32_16x16x32_bf16 v[52:55], v[104:107], v[72:75], 0\n"
    "v_cvt_pk_bf16_f32 v56, v56, v57\n"
    "v_cvt_pk_bf16_f32 v60, v60, v61\n"
    "v_mfma_f32_16x16x32_bf16 v[48:51], v[92:95], v[76:79], v[48:51]\n"
    "ds_write2_b32 v5, v56, v60 offset1:160\n"
    "v_mfma_f32_16x16x32_bf16 v[52:55], v[108:111], v[76:79], v[52:55]\n"
    "s_waitcnt lgkmcnt(1)\n"
    "v_mfma_f32_16x16x32_bf16 v[48:51], v[96:99], v[80:83], v[48:51]\n"
    "v_mfma_f32_16x16x32_bf16 v[52:55], v[112:115], v[80:83], v[52:55]\n"
    "v_mfma_f32_16x16x32_bf16 v[48:51], v[100:103], v[84:87], v[48:51]\n"
    "v_mfma_f32_16x16x32_bf16 v[52:55], v[116:119], v[84:87], v[52:55]\n"
    "s_nop 0\n"
    "s_nop 0\n"
    "v_smfmac_f32_16x16x64_bf16 v[64:67], v[24:27], v[144:151], v1\n"
    "s_waitcnt lgkmcnt(0)\n"
    "s_barrier\n"
    "v_smfmac_f32_16x16x64_bf16 v[68:71], v[24:27], v[208:215], v1\n"
    "ds_read_b64_tr_b16 v[88:89], v3 offset:0\n"
    "v_max3_f32 v60, v48, v49, v50\n"
    "v_max3_f32 v61, v52, v53, v54\n"
    "v_smfmac_f32_16x16x64_bf16 v[64:67], v[28:31], v[152:159], v1\n"
    "ds_read_b64_tr_b16 v[90:91], v3 offset:640\n"
    "v_max3_f32 v60, v60, v51, v55\n"
    "v_max_f32 v60, v60, v61\n"
    "v_mov_b32 v61, v60\n"
    "v_smfmac_f32_16x16x64_bf16 v[68:71], v[28:31], v[216:223], v1\n"
    "ds_read_b64_tr_b16 v[92:93], v3 offset:1280\n"
    "ds_read_b64_tr_b16 v[94:95], v3 offset:1920\n"
    "v_permlane16_swap_b32 v60, v61\n"
    "v_max_f32 v60, v60, v61\n"
    "v_mov_b32 v61, v60\n"
    "v_smfmac_f32_16x16x64_bf16 v[64:67], v[32:35], v[160:167], v1\n"
    "ds_read_b64_tr_b16 v[96:97], v3 offset:2560\n"
    "ds_read_b64_tr_b16 v[98:99], v3 offset:3200\n"
    "v_permlane32_swap_b32 v60, v61\n"
    "v_max_f32 v60, v60, v61\n"
    "v_mul_f32 v9, 0xbe0293ee, v60\n"
    "v_smfmac_f32_16x16x64_bf16 v[68:71], v[32:35], v[224:231], v1\n"
    "ds_read_b64_tr_b16 v[100:101], v3 offset:3840\n"
    "v_fmamk_f32 v48, v48, 0x3e0293ee, v9\n"
    "v_fmamk_f32 v49, v49, 0x3e0293ee, v9\n"
    "v_smfmac_f32_16x16x64_bf16 v[64:67], v[36:39], v[168:175], v1\n"
    "ds_read_b64_tr_b16 v[102:103], v3 offset:4480\n"
    "s_waitcnt lgkmcnt(4)\n"
    "v_fmamk_f32 v50, v50, 0x3e0293ee, v9\n"
    "v_fmamk_f32 v51, v51, 0x3e0293ee, v9\n"
    "v_mfma_f32_16x16x32_bf16 v[56:59], v[88:91], v[72:75], 0\n"
    "v_exp_f32 v48, v48\n"
    "v_mfma_f32_16x16x32_bf16 v[56:59], v[92:95], v[76:79], v[56:59]\n"
    "s_waitcnt lgkmcnt(2)\n"
    "v_exp_f32 v49, v49\n"
    "v_mfma_f32_16x16x32_bf16 v[56:59], v[96:99], v[80:83], v[56:59]\n"
    "s_waitcnt lgkmcnt(0)\n"
    "v_exp_f32 v50, v50\n"
    "v_mfma_f32_16x16x32_bf16 v[56:59], v[100:103], v[84:87], v[56:59]\n"
    "v_exp_f32 v51, v51\n"
    "v_smfmac_f32_16x16x64_bf16 v[68:71], v[36:39], v[232:239], v1\n"
    "ds_read_b128 v[104:107], v7 offset:0\n"
    "v_fmamk_f32 v52, v52, 0x3e0293ee, v9\n"
    "v_fmamk_f32 v53, v53, 0x3e0293ee, v9\n"
    "v_smfmac_f32_16x16x64_bf16 v[64:67], v[40:43], v[176:183], v1\n"
    "ds_read_b128 v[108:111], v7 offset:1024\n"
    "v_fmamk_f32 v54, v54, 0x3e0293ee, v9\n"
    "v_fmamk_f32 v55, v55, 0x3e0293ee, v9\n"
    "v_smfmac_f32_16x16x64_bf16 v[68:71], v[40:43], v[240:247], v1\n"
    "ds_read_b128 v[112:115], v7 offset:2048\n"
    "v_fmamk_f32 v56, v56, 0x3e0293ee, v9\n"
    "v_exp_f32 v52, v52\n"
    "v_smfmac_f32_16x16x64_bf16 v[64:67], v[44:47], v[184:191], v1\n"
    "ds_read_b128 v[116:119], v7 offset:3072\n"
    "v_fmamk_f32 v57, v57, 0x3e0293ee, v9\n"
    "v_exp_f32 v53, v53\n"
    "v_smfmac_f32_16x16x64_bf16 v[68:71], v[44:47], v[248:255], v1\n"
    "ds_read_b128 v[16:19], v7 offset:4096\n"
    "v_max_f32 v58, v56, v57\n"
    "v_exp_f32 v54, v54\n"
    "v_cmp_lt_f32 vcc, 0x41800000, v58\n"
    "v_exp_f32 v55, v55\n"
    "v_cvt_pk_bf16_f32 v60, v48, v49\n"
    "v_cvt_pk_bf16_f32 v61, v50, v51\n"
    "v_cvt_pk_bf16_f32 v62, v52, v53\n"
    "v_cvt_pk_bf16_f32 v63, v54, v55\n"
    "v_mov_b64 v[84:85], 0\n"
    "v_mov_b64 v[86:87], 0\n"
    "v_mfma_f32_16x16x32_bf16 v[80:83], v[12:15], v[60:63], 0\n"
    "v_add_f32 v64, v64, v66\n"
    "v_add_f32 v65, v65, v67\n"
    "v_mfma_f32_16x16x32_bf16 v[72:75], v[120:123], v[60:63], 0\n"
    "v_add_f32 v68, v68, v70\n"
    "v_add_f32 v69, v69, v71\n"
    "v_mfma_f32_16x16x32_bf16 v[76:79], v[124:127], v[60:63], 0\n"
    "ds_read_b128 v[20:23], v7 offset:5120\n"
    "s_cbranch_vccnz LHEAD_RESC_%=\n"
    "LHEAD_RESCD_%=:\n"
    "v_exp_f32 v56, v56\n"
    "v_exp_f32 v57, v57\n"
    "v_cvt_pk_bf16_f32 v10, v64, v65\n"
    "v_cvt_pk_bf16_f32 v11, v68, v69\n"
    "v_cvt_pk_bf16_f32 v85, v56, v57\n"
    "v_cvt_pk_bf16_f32 v56, v56, v57\n"
    "v_mov_b32 v57, 0\n"
    "v_mov_b32 v58, 0\n"
    "v_mfma_f32_16x16x32_bf16 v[80:83], v[12:15], v[84:87], v[80:83]\n"
    "ds_read_b128 v[24:27], v7 offset:6144\n"
    "v_mov_b64 v[64:65], 0\n"
    "v_mfma_f32_16x16x32_bf16 v[76:79], v[10:13], v[84:87], v[76:79]\n"
    "ds_read_b128 v[28:31], v7 offset:7168\n"
    "v_mov_b64 v[66:67], 0\n"
    "v_mfma_f32_16x16x32_bf16 v[72:75], v[10:13], v[56:59], v[72:75]\n"
    "ds_read_b128 v[88:91], v7 offset:8192\n"
    "v_mov_b64 v[68:69], 0\n"
    "v_mov_b64 v[70:71], 0\n"
    "v_rcp_f32 v9, v80\n"
    "ds_read_b128 v[92:95], v7 offset:9216\n"
    "v_mov_b64 v[60:61], 0\n"
    "v_mov_b64 v[62:63], 0\n"
    "v_mov_b64 v[84:85], 0\n"
    "v_cndmask_b32 v72, v72, v76, s[64:65]\n"
    "v_cndmask_b32 v73, v73, v77, s[64:65]\n"
    "v_cndmask_b32 v74, v74, v78, s[64:65]\n"
    "v_cndmask_b32 v75, v75, v79, s[64:65]\n"
    "v_pk_mul_f32 v[72:73], v[72:73], v[8:9] op_sel:[0,1] op_sel_hi:[1,1]\n"
    "v_pk_mul_f32 v[74:75], v[74:75], v[8:9] op_sel:[0,1] op_sel_hi:[1,1]\n"
    "v_cvt_pk_bf16_f32 v48, v72, v73\n"
    "v_cvt_pk_bf16_f32 v49, v74, v75\n"
    "ds_write_b64 v6, v[48:49]\n"
    "v_mov_b64 v[72:73], 0\n"
    "v_mov_b64 v[74:75], 0\n"
    "v_mov_b64 v[76:77], 0\n"
    "v_mov_b64 v[78:79], 0\n"
    "v_mov_b64 v[80:81], 0\n"
    "v_mov_b64 v[82:83], 0\n"
    "v_mov_b64 v[86:87], 0\n"
    "v_mov_b64 v[56:57], 0\n"
    "v_mov_b64 v[58:59], 0\n"
    "buffer_load_dword v9, off, s[40:43], s53 offset:1024 sc1\n"
    "s_waitcnt lgkmcnt(0)\n"
    "s_barrier\n"
    "ds_read_b128 v[48:51], v0 offset:30720\n"
    "ds_read_b128 v[52:55], v0 offset:31744\n"
    "s_add_u32 s52, s54, 65536\n"
    "s_waitcnt lgkmcnt(1)\n"
    "v_smfmac_f32_16x16x64_bf16 v[72:75], v[48:51], a[0:7], v1\n"
    "ds_read_b128 v[96:99], v7 offset:10240\n"
    "v_smfmac_f32_16x16x64_bf16 v[76:79], v[48:51], a[16:23], v1\n"
    "s_waitcnt lgkmcnt(1)\n"
    "ds_read_b128 v[100:103], v7 offset:11264\n"
    "v_smfmac_f32_16x16x64_bf16 v[72:75], v[52:55], a[8:15], v1\n"
    "ds_read_b128 v[32:35], v7 offset:12288\n"
    "v_smfmac_f32_16x16x64_bf16 v[76:79], v[52:55], a[24:31], v1\n"
    "ds_read_b128 v[36:39], v7 offset:13312\n"
    "v_smfmac_f32_16x16x64_bf16 v[80:83], v[48:51], a[32:39], v1\n"
    "ds_read_b128 v[40:43], v7 offset:14336\n"
    "v_smfmac_f32_16x16x64_bf16 v[80:83], v[52:55], a[40:47], v1\n"
    "ds_read_b128 v[44:47], v7 offset:15360\n"
    "s_nop 0\n"
    "v_add_f32 v72, v72, v74\n"
    "v_add_f32 v73, v73, v75\n"
    "v_smfmac_f32_16x16x64_bf16 v[84:87], v[48:51], v[104:111], v1\n"
    "ds_read_b128 v[120:123], v7 offset:16384\n"
    "v_add_f32 v76, v76, v78\n"
    "v_add_f32 v77, v77, v79\n"
    "v_smfmac_f32_16x16x64_bf16 v[84:87], v[52:55], v[112:119], v1\n"
    "ds_read_b128 v[124:127], v7 offset:17408\n"
    "v_cvt_pk_bf16_f32 v72, v72, v73\n"
    "v_cvt_pk_bf16_f32 v73, v76, v77\n"
    "v_smfmac_f32_16x16x64_bf16 v[56:59], v[48:51], v[16:23], v1\n"
    "ds_read_b128 v[104:107], v7 offset:18432\n"
    "v_add_f32 v80, v80, v82\n"
    "v_add_f32 v81, v81, v83\n"
    "v_smfmac_f32_16x16x64_bf16 v[56:59], v[52:55], v[24:31], v1\n"
    "ds_read_b128 v[108:111], v7 offset:19456\n"
    "v_cvt_pk_bf16_f32 v74, v80, v81\n"
    "v_add_f32 v84, v84, v86\n"
    "v_smfmac_f32_16x16x64_bf16 v[60:63], v[48:51], v[88:95], v1\n"
    "s_waitcnt lgkmcnt(4)\n"
    "v_add_f32 v85, v85, v87\n"
    "v_cvt_pk_bf16_f32 v75, v84, v85\n"
    "v_smfmac_f32_16x16x64_bf16 v[60:63], v[52:55], v[96:103], v1\n"
    "buffer_store_dwordx4 v[72:75], v8, s[40:43], s54 offen sc1\n"
    "v_add_f32 v56, v56, v58\n"
    "v_smfmac_f32_16x16x64_bf16 v[64:67], v[48:51], v[32:39], v1\n"
    "v_add_f32 v57, v57, v59\n"
    "v_smfmac_f32_16x16x64_bf16 v[64:67], v[52:55], v[40:47], v1\n"
    "s_waitcnt lgkmcnt(0)\n"
    "v_cvt_pk_bf16_f32 v56, v56, v57\n"
    "v_smfmac_f32_16x16x64_bf16 v[68:71], v[48:51], v[120:127], v1\n"
    "v_add_f32 v60, v60, v62\n"
    "v_add_f32 v61, v61, v63\n"
    "v_smfmac_f32_16x16x64_bf16 v[68:71], v[52:55], v[104:111], v1\n"
    "v_cvt_pk_bf16_f32 v57, v60, v61\n"
    "s_nop 0\n"
    "v_add_f32 v64, v64, v66\n"
    "v_add_f32 v65, v65, v67\n"
    "v_cvt_pk_bf16_f32 v58, v64, v65\n"
    "s_nop 2\n"
    "v_add_f32 v68, v68, v70\n"
    "v_add_f32 v69, v69, v71\n"
    "v_cvt_pk_bf16_f32 v59, v68, v69\n"
    "buffer_store_dwordx4 v[56:59], v8, s[40:43], s52 offen sc1\n"
    "s_memtime s[50:51]\n"
    "s_nop 0\n"
    "v_cvt_f32_u32 v20, s82\n"
    "s_waitcnt vmcnt(2) lgkmcnt(0)\n"
    "s_sub_u32 s50, s50, s85\n"
    "v_rcp_f32 v20, v20\n"
    "v_cvt_f32_u32 v21, s50\n"
    "v_readfirstlane_b32 s96, v9\n"
    "v_mul_f32 v20, 0x43800000, v20\n"
    "v_fma_f32 v21, v21, v20, 0.5\n"
    "v_cvt_u32_f32 v21, v21\n"
    "v_add_u32 v20, s48, v21\n"
    "s_nop 0\n"
    "v_readfirstlane_b32 s50, v20\n"
    "s_cmp_lg_u32 s60, 0\n"
    "s_cbranch_scc1 LHEAD_ND_%=\n"
    "s_lshl_b32 s52, s61, 5\n"
    "s_add_u32 s52, s52, s53\n"
    "s_mov_b64 exec, 1\n"
    "buffer_store_dwordx2 v[20:21], off, s[40:43], s52 sc1\n"
    "s_mov_b64 exec, -1\n"
    "LHEAD_ND_%=:\n"
    "s_add_u32 s56, s56, 0x2000\n"
    "s_cmp_ge_u32 s44, 2\n"
    "s_cbranch_scc1 LHEAD_KVG_%=\n"
    "s_add_u32 s45, s44, 1\n"
    "v_and_b32 v20, 0x30, v0\n"
    "v_lshlrev_b32 v20, 3, v20\n"
    "s_and_b32 s52, s45, 1\n"
    "s_lshl_b32 s52, s52, 15\n"
    "s_add_u32 s52, s52, s90\n"
    "s_add_u32 s57, s98, s45\n"
    "s_add_u32 s57, s57, 1\n"
    "s_mov_b32 s58, 0\n"
    "LHEAD_P4_%=:\n"
    "buffer_load_dwordx2 v[16:17], v20, s[40:43], s52 offen sc1\n"
    "s_add_u32 s58, s58, 1\n"
    "s_cmp_gt_u32 s58, 0x40000\n"
    "s_cbranch_scc1 LHEAD_FAIL_%=\n"
    "s_waitcnt vmcnt(0)\n"
    "v_subrev_u32 v18, s57, v16\n"
    "v_lshrrev_b32 v18, 6, v18\n"
    "v_xor_b32 v19, s99, v17\n"
    "v_or_b32 v18, v18, v19\n"
    "v_cmp_ne_u32 vcc, 0, v18\n"
    "s_nop 1\n"
    "s_cbranch_vccnz LHEAD_P4_%=\n"
    "LHEAD_KVG_%=:\n"
    "s_add_u32 m0, s62, 8192\n"
    "s_nop 0\n"
    "buffer_load_dwordx4 v0, s[40:43], s56 offen sc1 lds\n"
    "buffer_load_dwordx4 v0, s[40:43], s56 offen offset:1024 sc1 lds\n"
    "s_add_u32 s52, s56, 0x80000\n"
    "buffer_load_dwordx4 v[120:123], v0, s[40:43], s52 offen sc1\n"
    "buffer_load_dwordx4 v[124:127], v0, s[40:43], s52 offen offset:1024 sc1\n"
    "ds_read_b128 v[72:75], v7 offset:20480\n"
    "ds_read_b128 v[76:79], v7 offset:21504\n"
    "ds_read_b128 v[80:83], v7 offset:22528\n"
    "ds_read_b128 v[84:87], v7 offset:23552\n"
    "ds_read_b128 v[88:91], v7 offset:24576\n"
    "ds_read_b128 v[92:95], v7 offset:25600\n"
    "ds_read_b128 v[96:99], v7 offset:26624\n"
    "ds_read_b128 v[100:103], v7 offset:27648\n"
    "ds_read_b128 v[104:107], v7 offset:28672\n"
    "ds_read_b128 v[108:111], v7 offset:29696\n"
    "ds_read_b128 v[112:115], v7 offset:30720\n"
    "ds_read_b128 v[116:119], v7 offset:31744\n"
    "v_mov_b64 v[48:49], 0\n"
    "v_mov_b64 v[50:51], 0\n"
    "v_mov_b64 v[52:53], 0\n"
    "v_mov_b64 v[54:55], 0\n"
    "v_mov_b64 v[56:57], 0\n"
    "v_mov_b64 v[58:59], 0\n"
    "v_mov_b64 v[60:61], 0\n"
    "v_mov_b64 v[62:63], 0\n"
    "v_mov_b64 v[64:65], 0\n"
    "v_mov_b64 v[66:67], 0\n"
    "v_mov_b64 v[68:69], 0\n"
    "v_mov_b64 v[70:71], 0\n"
    "s_sub_u32 s52, s50, s48\n"
    "s_add_u32 s46, s46, 3\n"
    "s_min_u32 s46, s46, s52\n"
    "s_mov_b32 s45, 0\n"
    "s_mov_b32 s58, 0\n"
    "LHEAD_SCH_%=:\n"
    "s_sub_u32 s52, s96, s92\n"
    "s_cmp_lt_u32 s52, 0x80000\n"
    "s_cbranch_scc1 LHEAD_SOK_%=\n"
    "s_sleep 3\n"
    "buffer_load_dword v16, off, s[40:43], s53 offset:1024 sc1\n"
    "s_add_u32 s58, s58, 1\n"
    "s_waitcnt vmcnt(0)\n"
    "v_readfirstlane_b32 s96, v16\n"
    "s_cmp_lt_u32 s58, 0x2000\n"
    "s_cbranch_scc1 LHEAD_SCH_%=\n"
    "s_branch LHEAD_SLOW_%=\n"
    "LHEAD_SOK_%=:\n"
    "s_sub_u32 s52, s96, s50\n"
    "s_cmp_ge_i32 s52, s97\n"
    "s_cbranch_scc1 LHEAD_GO_%=\n"
    "LHEAD_SLOW_%=:\n"
    "s_add_u32 s96, s50, 45\n"
    "s_mov_b32 s45, 1\n"
    "LHEAD_GO_%=:\n"
    "s_add_u32 s66, s96, s59\n"
    "v_and_b32 v9, 0x310, v0\n"
    "v_and_b32 v19, 0x20, v0\n"
    "v_lshl_or_b32 v9, v19, 2, v9\n"
    "v_and_b32 v19, 0xc0, v0\n"
    "v_lshrrev_b32 v19, 1, v19\n"
    "v_or_b32 v9, v9, v19\n"
    "s_add_u32 s44, s44, 1\n"
    "s_cmp_eq_u32 s44, 63\n"
    "s_cbranch_scc1 LHEAD_END_%=\n"
    "s_lshr_b32 s52, s44, 2\n"
    "s_lshl_b32 s53, s52, 14\n"
    "s_and_b32 s52, s44, 3\n"
    "s_lshl_b32 s55, s52, 16\n"
    "s_add_u32 s55, s55, s70\n"
    "s_lshl_b32 s52, s52, 11\n"
    "s_add_u32 s53, s53, s52\n"
    "s_add_u32 s53, s53, s71\n"
    "s_add_u32 s53, s53, 0x100000\n"
    "s_and_b32 s52, s44, 1\n"
    "s_lshl_b32 s54, s52, 19\n"
    "s_add_u32 s54, s54, s71\n"
    "s_add_u32 m0, s62, 0\n"
    "s_mov_b32 s48, s66\n"
    "s_mov_b32 s47, 0\n"
    "s_or_b32 s52, s60, s61\n"
    "s_cbranch_scc1 LHEAD_N1_%=\n"
    "s_cmp_eq_u32 s45, 0\n"
    "s_cbranch_scc1 LHEAD_N1_%=\n"
    "s_add_u32 s52, s48, s46\n"
    "s_add_u32 s52, s52, 40\n"
    "v_mov_b32 v10, s52\n"
    "v_lshlrev_b32 v11, 3, v0\n"
    "s_mov_b64 exec, 3\n"
    "buffer_store_dword v10, v11, s[40:43], s53 offen offset:1024 sc1\n"
    "s_mov_b64 exec, -1\n"
    "LHEAD_N1_%=:\n"
    "s_sub_u32 s52, s66, s84\n"
    "s_mov_b32 s84, s66\n"
    "s_mul_i32 s52, s52, s82\n"
    "s_ashr_i32 s52, s52, 8\n"
    "s_add_u32 s83, s83, s52\n"
    "s_mov_b32 s85, s83\n"
    "LHEAD_TW_%=:\n"
    "s_memrealtime s[68:69]\n"
    "s_memtime s[86:87]\n"
    "s_waitcnt lgkmcnt(0)\n"
    "s_sub_u32 s52, s84, s68\n"
    "s_mul_i32 s52, s52, s82\n"
    "s_ashr_i32 s52, s52, 8\n"
    "s_sub_u32 s88, s85, s86\n"
    "s_min_i32 s88, s88, s52\n"
    "s_lshr_b32 s87, s82, 6\n"
    "s_sub_u32 s52, s52, s87\n"
    "s_max_i32 s88, s88, s52\n"
    "s_add_u32 s85, s88, s86\n"
    "s_cmp_gt_i32 s88, 190\n"
    "s_cbranch_scc1 LHEAD_TW_%=\n"
    "LHEAD_TF_%=:\n"
    "s_sub_u32 s88, s88, 32\n"
    "s_cmp_gt_i32 s88, 60\n"
    "s_cbranch_scc1 LHEAD_TF_%=\n"
    "buffer_load_dwordx4 v9, s[40:43], s55 offen sc1 lds\n"
    "s_sleep 2\n"
    "s_nop 9\n"
    "buffer_load_dwordx4 v9, s[40:43], s55 offen offset:1024 sc1 lds\n"
    "s_sub_u32 s52, s85, s83\n"
    "s_max_i32 s52, s52, -64\n"
    "s_min_i32 s52, s52, 64\n"
    "s_mul_i32 s52, s52, 3\n"
    "s_ashr_i32 s52, s52, 3\n"
    "s_add_u32 s82, s82, s52\n"
    "s_mov_b32 s83, s85\n"
    "s_branch LHEAD_LOOP_%=\n"
    "LHEAD_RETRY_%=:\n"
    "s_memrealtime s[48:49]\n"
    "s_memtime s[86:87]\n"
    "s_nop 15\n"
    "s_waitcnt vmcnt(0) lgkmcnt(0)\n"
    "s_mov_b32 s85, s86\n"
    "s_add_u32 s48, s48, 3\n"
    "s_or_b32 s52, s60, s61\n"
    "s_cbranch_scc1 LHEAD_N2_%=\n"
    "s_add_u32 s52, s48, s46\n"
    "s_add_u32 s52, s52, 40\n"
    "v_mov_b32 v10, s52\n"
    "v_lshlrev_b32 v11, 3, v0\n"
    "s_mov_b64 exec, 3\n"
    "buffer_store_dword v10, v11, s[40:43], s53 offen offset:1024 sc1\n"
    "s_mov_b64 exec, -1\n"
    "LHEAD_N2_%=:\n"
    "s_barrier\n"
    "v_and_b32 v9, 0x310, v0\n"
    "v_and_b32 v19, 0x20, v0\n"
    "v_lshl_or_b32 v9, v19, 2, v9\n"
    "v_and_b32 v19, 0xc0, v0\n"
    "v_lshrrev_b32 v19, 1, v19\n"
    "v_or_b32 v9, v9, v19\n"
    "s_add_u32 m0, s62, 0\n"
    "s_add_u32 s47, s47, 1\n"
    "s_cmp_ge_u32 s47, 0x8000\n"
    "s_cbranch_scc1 LHEAD_FAIL_%=\n"
    "buffer_load_dwordx4 v9, s[40:43], s55 offen sc1 lds\n"
    "buffer_load_dwordx4 v9, s[40:43], s55 offen offset:1024 sc1 lds\n"
    "ds_read_b128 v[72:75], v7 offset:20480\n"
    "ds_read_b128 v[76:79], v7 offset:21504\n"
    "ds_read_b128 v[80:83], v7 offset:22528\n"
    "ds_read_b128 v[84:87], v7 offset:23552\n"
    "ds_read_b128 v[88:91], v7 offset:24576\n"
    "ds_read_b128 v[92:95], v7 offset:25600\n"
    "ds_read_b128 v[96:99], v7 offset:26624\n"
    "ds_read_b128 v[100:103], v7 offset:27648\n"
    "ds_read_b128 v[104:107], v7 offset:28672\n"
    "ds_read_b128 v[108:111], v7 offset:29696\n"
    "ds_read_b128 v[112:115], v7 offset:30720\n"
    "ds_read_b128 v[116:119], v7 offset:31744\n"
    "v_mov_b64 v[48:49], 0\n"
    "v_mov_b64 v[50:51], 0\n"
    "v_mov_b64 v[52:53], 0\n"
    "v_mov_b64 v[54:55], 0\n"
    "v_mov_b64 v[56:57], 0\n"
    "v_mov_b64 v[58:59], 0\n"
    "v_mov_b64 v[60:61], 0\n"
    "v_mov_b64 v[62:63], 0\n"
    "v_mov_b64 v[64:65], 0\n"
    "v_mov_b64 v[66:67], 0\n"
    "v_mov_b64 v[68:69], 0\n"
    "v_mov_b64 v[70:71], 0\n"
    "s_branch LHEAD_LOOP_%=\n"
    "LHEAD_RESC_%=:\n"
    "v_max_f32 v58, 0, v58\n"
    "v_mov_b32 v59, v58\n"
    "s_nop 1\n"
    "v_permlane16_swap_b32 v58, v59\n"
    "v_max_f32 v58, v58, v59\n"
    "v_mov_b32 v59, v58\n"
    "s_nop 1\n"
    "v_permlane32_swap_b32 v58, v59\n"
    "v_max_f32 v58, v58, v59\n"
    "v_mov_b32 v59, 0\n"
    "v_sub_f32 v56, v56, v58\n"
    "v_sub_f32 v57, v57, v58\n"
    "v_exp_f32 v58, -v58\n"
    "s_nop 7\n"
    "v_mul_f32 v72, v72, v58\n"
    "v_mul_f32 v73, v73, v58\n"
    "v_mul_f32 v74, v74, v58\n"
    "v_mul_f32 v75, v75, v58\n"
    "v_mul_f32 v76, v76, v58\n"
    "v_mul_f32 v77, v77, v58\n"
    "v_mul_f32 v78, v78, v58\n"
    "v_mul_f32 v79, v79, v58\n"
    "v_mul_f32 v80, v80, v58\n"
    "v_mul_f32 v81, v81, v58\n"
    "v_mul_f32 v82, v82, v58\n"
    "v_mul_f32 v83, v83, v58\n"
    "s_nop 1\n"
    "s_branch LHEAD_RESCD_%=\n"
    "LHEAD_FAIL_%=:\n"
    "v_mov_b32 v16, 3\n"
    "s_mov_b32 s52, 0x40000\n"
    "buffer_store_dword v16, off, s[40:43], s52 sc1\n"
    "LHEAD_END_%=:\n"
    "s_waitcnt vmcnt(0) lgkmcnt(0)\n"
    "s_endpgm\n"
      :
      : "s"(p.ws), "s"(h), "s"(w), "s"(p.V2 - p.EA), "s"(p.mem0), "s"(p.comm), "s"(bid & 3), "s"(p.Wq), "s"(p.Wk), "s"(p.Wv), "s"(p.Wo), "s"(p.V3), "s"(bid),
        "{v0}"(lane * 16), "{v1}"(sidx), "{v2}"(trq), "{v3}"(trk), "{v4}"(qwr), "{v5}"(kwr), "{v6}"(owr), "{v7}"(wob), "{v8}"(poff), "{v9}"(sqkv), "{v10}"(swo), "{v11}"(rqkv), "{v14}"(rwo), "{v15}"((tc * DM + 128 * w + 2 * (2 * g + bc)) * 2)
      : "memory", "vcc", "scc", CLOB_V, CLOB_A, CLOB_S);
  __builtin_unreachable();
}

#define DV1 11
#define DV2 41
extern "C" void kernel_launch(void* const* d_in, const int* in_sizes, int n_in, void* d_out, int out_size, void* d_ws,
                              size_t ws_size, hipStream_t stream) {
  Params p;
  p.x = (cus)d_in[0]; p.mem0 = (cus)d_in[1]; p.Wq = (cus)d_in[2]; p.Wk = (cus)d_in[3]; p.Wv = (cus)d_in[4]; p.Wo = (cus)d_in[5];
  p.w1 = (cus)d_in[8]; p.b1 = (cus)d_in[9]; p.w2 = (cus)d_in[10]; p.b2 = (cus)d_in[11]; p.c = (cus)d_in[12]; p.wmod = (cus)d_in[13];
  p.out = (unsigned short*)d_out;
  p.ws = (char*)d_ws;
  p.comm = 0;
  p.V1 = DV1; p.V2 = DV2; p.V3 = 5; p.EA = 6; p.pad = 0;
  (void)hipFuncSetAttribute((const void*)k_fused, hipFuncAttributeMaxDynamicSharedMemorySize, LDS_BYTES);
  k_fused<<<256, 256, LDS_BYTES, stream>>>(p);
}
```
